```python
import torch

def _softmax(x, axis=-1):
    return torch.softmax(x, dim=axis)
def _gelu(x):
    return torch.nn.functional.gelu(x, approximate="tanh")
def _silu(x):
    return torch.nn.functional.silu(x)
def _sigmoid(x):
    return torch.sigmoid(x)
def _layernorm(x, axis=-1, eps=1e-5):
    m = x.mean(dim=axis, keepdim=True)
    v = x.var(dim=axis, keepdim=True, unbiased=False)
    return (x - m) / torch.sqrt(v + eps)
def _scatter_add(tgt, idx, src, axis=0):
    return tgt.clone().index_add_(axis, idx, src)
def _segment_sum(src, seg, n):
    out = torch.zeros((n,) + src.shape[1:], dtype=src.dtype)
    return out.index_add_(0, seg, src)
def _pad(x, pw):
    flat = tuple(v for pair in reversed(pw) for v in pair)
    return torch.nn.functional.pad(x, flat)

kernel_name = 'gated_mix__ssm_conv_s4__dense_activation_relu__prompt'

def setup_inputs():

    _w = (lambda a: (lambda t: t.to(torch.bfloat16) if t.is_floating_point() else t)(torch.as_tensor(a)))
    def _A_setup():
        B, L, D, N = 1, 2048, 640, 192
        A_re = -(lambda _x: torch.exp(torch.as_tensor(_x)))((lambda *s: torch.randn(*s, dtype=torch.bfloat16) * 6.4)(D, N))
        A_im = torch.pi * (lambda *s: torch.randn(*s, dtype=torch.bfloat16) * 0.1)(D, N)
        B_re = (lambda *s: torch.randn(*s, dtype=torch.bfloat16) * 0.2)(D, N)
        B_im = (lambda *s: torch.randn(*s, dtype=torch.bfloat16) * 0.2)(D, N)
        C_re = (lambda *s: torch.randn(*s, dtype=torch.bfloat16) * 0.2)(D, N)
        C_im = (lambda *s: torch.randn(*s, dtype=torch.bfloat16) * 0.2)(D, N)
        log_dt = (lambda *s: torch.randn(*s, dtype=torch.bfloat16) * 0.1)(D) * 0.5 - 4.0
        Dskip = (lambda *s: torch.randn(*s, dtype=torch.bfloat16) * 0.1)(D)
        return {
            "u": _w((lambda *s: torch.randn(*s, dtype=torch.bfloat16) * 0.05)(B, L, D)),
            "A_re": _w(A_re),
            "A_im": _w(A_im),
            "B_re": _w(B_re),
            "B_im": _w(B_im),
            "C_re": _w(C_re),
            "C_im": _w(C_im),
            "log_dt": _w(log_dt),
            "Dskip": _w(Dskip),
        }
    _A_in = _A_setup()

    def _B_setup():
        B, S, D, Dff = 1, 2048, 640, 1536
        return {
            "x": _w((lambda *s: torch.randn(*s, dtype=torch.bfloat16) * 0.05)(B, S, D)),
            "w1": _w((lambda *s: torch.randn(*s, dtype=torch.bfloat16) * 0.05)(D, Dff)),
            "w2": _w((lambda *s: torch.randn(*s, dtype=torch.bfloat16) * 0.05)(Dff, D)),
        }
    _B_in = _B_setup()

    return {
        "A_u": _A_in["u"],
        "A_A_re": _A_in["A_re"],
        "A_A_im": _A_in["A_im"],
        "A_B_re": _A_in["B_re"],
        "A_B_im": _A_in["B_im"],
        "A_C_re": _A_in["C_re"],
        "A_C_im": _A_in["C_im"],
        "A_log_dt": _A_in["log_dt"],
        "A_Dskip": _A_in["Dskip"],
        "B_x": _B_in["x"],
        "B_w1": _B_in["w1"],
        "B_w2": _B_in["w2"],
        "gate_w": _w((lambda *s: torch.randn(*s, dtype=torch.bfloat16) * 0.05)(640) * 16.0),
    }

def reference(A_u, A_A_re, A_A_im, A_B_re, A_B_im, A_C_re, A_C_im, A_log_dt, A_Dskip, B_x, B_w1, B_w2, gate_w):
    def _A_ref(u, A_re, A_im, B_re, B_im, C_re, C_im, log_dt, Dskip):
        Bsz, L, D = u.shape
        dt = torch.exp(log_dt)[:, None]
        A = A_re + 1j * A_im
        Bc = B_re + 1j * B_im
        Cc = C_re + 1j * C_im
        logdA = dt * A
        dB = dt * Bc
        l = (lambda x, dt: x.to(dt))(torch.arange(L), A_re.dtype)
        P = torch.exp(logdA[:, :, None] * l[None, None, :])
        K = torch.einsum("dn,dnl,dn->dl", Cc, P, dB).real
        L2 = 2 * L
        Kp = _pad(K, ((0, 0), (0, L)))
        up = (lambda x, *d: x.permute(*d))(u, 0, 2, 1)
        up = _pad(up, ((0, 0), (0, 0), (0, L)))
        Kf = (lambda x, axes=(-2,-1): torch.fft.rfft2(x.float(), dim=axes))(Kp[:, None, :], axes=(-2, -1))
        Uf = (lambda x, axes=(-2,-1): torch.fft.rfft2(x.float(), dim=axes))(up[:, :, None, :], axes=(-2, -1))
        Yf = Uf * Kf[None, :, :, :]
        y = (lambda x, s, axes=(-2,-1): torch.fft.irfft2(x, s=s, dim=axes).to(torch.bfloat16))(Yf, (1, L2), axes=(-2, -1))[:, :, 0, :L]
        y = (lambda x, *d: x.permute(*d))(y, 0, 2, 1) + u * Dskip[None, None, :]
        return y

    def _B_ref(x, w1, w2):
        h = torch.einsum("bsd,df->bsf", x, w1)
        zero = torch.zeros_like(h)
        h = torch.maximum(h, zero)
        return torch.einsum("bsf,fd->bsd", h, w2)

    x = A_u
    g = _sigmoid((x * gate_w).sum(-1, keepdims=True))
    a = _A_ref(x, A_A_re, A_A_im, A_B_re, A_B_im, A_C_re, A_C_im, A_log_dt, A_Dskip)
    b = _B_ref(x, B_w1, B_w2)
    return g * a + (1 - g) * b
```

```cpp
#include <hip/hip_runtime.h>
#include <hip/hip_bf16.h>
#include <stdint.h>

typedef unsigned short u16;
static constexpr int L = 2048, D = 640, N = 192, F = 1536;

__device__ __forceinline__ float bf2f(u16 x){ return __uint_as_float(((unsigned)x) << 16); }
__device__ __forceinline__ u16 f2bf(float f){ unsigned u = __float_as_uint(f); u = (u + 0x7FFF + ((u >> 16) & 1)) >> 16; return (u16)u; }
__device__ __forceinline__ float rbf(float f){ return bf2f(f2bf(f)); }

__global__ void kgen(const u16* Are, const u16* Aim, const u16* Bre, const u16* Bim, const u16* Cre, const u16* Cim,
                     const u16* logdt, float* K) {
  int d = blockIdx.x;
  __shared__ float sar[N], sai[N], scr[N], sci[N];
  float dt = rbf(expf(bf2f(logdt[d])));
  for (int n = threadIdx.x; n < N; n += blockDim.x) {
    float ar = bf2f(Are[d*N+n]), ai = bf2f(Aim[d*N+n]);
    float br = bf2f(Bre[d*N+n]) * dt, bi = bf2f(Bim[d*N+n]) * dt;
    float cr = bf2f(Cre[d*N+n]), ci = bf2f(Cim[d*N+n]);
    sar[n] = ar * dt; sai[n] = ai * dt;
    scr[n] = cr * br - ci * bi; sci[n] = cr * bi + ci * br;
  }
  __syncthreads();
  for (int l = threadIdx.x; l < L; l += blockDim.x) {
    float lf = rbf((float)l);
    float acc = 0.f;
    for (int n = 0; n < N; ++n) {
      float e = expf(sar[n] * lf);
      float s, c; sincosf(sai[n] * lf, &s, &c);
      acc += e * (scr[n] * c - sci[n] * s);
    }
    K[d*L + l] = acc;
  }
}

__global__ void conv(const u16* u, const float* K, const u16* Dskip, u16* a) {
  int d = blockIdx.x;
  __shared__ float su[L], sk[L];
  for (int t = threadIdx.x; t < L; t += blockDim.x) { su[t] = bf2f(u[t*D + d]); sk[t] = K[d*L + t]; }
  __syncthreads();
  float ds = bf2f(Dskip[d]);
  for (int t = threadIdx.x; t < L; t += blockDim.x) {
    float acc = 0.f;
    for (int l = 0; l <= t; ++l) acc += sk[l] * su[t - l];
    float y = rbf(acc);
    float sk2 = rbf(su[t] * ds);
    a[t*D + d] = f2bf(y + sk2);
  }
}

__global__ void gate(const u16* u, const u16* gw, float* g) {
  int t = blockIdx.x;
  float acc = 0.f;
  for (int d = threadIdx.x; d < D; d += 64) acc += rbf(bf2f(u[t*D+d]) * bf2f(gw[d]));
  for (int o = 32; o > 0; o >>= 1) acc += __shfl_xor(acc, o);
  if (threadIdx.x == 0) { float s = rbf(acc); g[t] = rbf(1.f / (1.f + expf(-s))); }
}

__global__ void gemm1(const u16* x, const u16* w1, u16* h) {
  int t = blockIdx.x;
  __shared__ float sx[D];
  for (int d = threadIdx.x; d < D; d += blockDim.x) sx[d] = bf2f(x[t*D+d]);
  __syncthreads();
  for (int f = threadIdx.x; f < F; f += blockDim.x) {
    float acc = 0.f;
    for (int d = 0; d < D; ++d) acc += sx[d] * bf2f(w1[d*F + f]);
    float hv = rbf(acc);
    h[t*F + f] = f2bf(fmaxf(hv, 0.f));
  }
}
__global__ void gemm2(const u16* h, const u16* w2, const u16* a, const float* g, u16* out) {
  int t = blockIdx.x;
  __shared__ float sh[F];
  for (int f = threadIdx.x; f < F; f += blockDim.x) sh[f] = bf2f(h[t*F+f]);
  __syncthreads();
  float gv = g[t];
  float og = rbf(1.f - gv);
  for (int d = threadIdx.x; d < D; d += blockDim.x) {
    float acc = 0.f;
    for (int f = 0; f < F; ++f) acc += sh[f] * bf2f(w2[f*D + d]);
    float b = rbf(acc);
    float av = bf2f(a[t*D+d]);
    out[t*D + d] = f2bf(rbf(gv * av) + rbf(og * b));
  }
}

extern "C" void kernel_launch(void* const* d_in, const int* in_sizes, int n_in,
                              void* d_out, int out_size, void* d_ws, size_t ws_size,
                              hipStream_t stream) {
  const u16* u = (const u16*)d_in[0];
  const u16* Are = (const u16*)d_in[1];
  const u16* Aim = (const u16*)d_in[2];
  const u16* Bre = (const u16*)d_in[3];
  const u16* Bim = (const u16*)d_in[4];
  const u16* Cre = (const u16*)d_in[5];
  const u16* Cim = (const u16*)d_in[6];
  const u16* logdt = (const u16*)d_in[7];
  const u16* Dskip = (const u16*)d_in[8];
  const u16* w1 = (const u16*)d_in[10];
  const u16* w2 = (const u16*)d_in[11];
  const u16* gw = (const u16*)d_in[12];
  char* ws = (char*)d_ws;
  float* K = (float*)ws; ws += (size_t)D * L * 4;
  u16* a = (u16*)ws; ws += (size_t)L * D * 2;
  u16* h = (u16*)ws; ws += (size_t)L * F * 2;
  float* g = (float*)ws; ws += L * 4;
  kgen<<<D, 256, 0, stream>>>(Are, Aim, Bre, Bim, Cre, Cim, logdt, K);
  conv<<<D, 256, 0, stream>>>(u, K, Dskip, a);
  gate<<<L, 64, 0, stream>>>(u, gw, g);
  gemm1<<<L, 256, 0, stream>>>(u, w1, h);
  gemm2<<<L, 256, 0, stream>>>(h, w2, a, g, (u16*)d_out);
}
```

```cpp
#include <hip/hip_runtime.h>
#include <stdint.h>

typedef unsigned short u16;
typedef unsigned u32;
typedef unsigned long long u64;
typedef float f32x4 __attribute__((ext_vector_type(4)));
typedef float f32x2 __attribute__((ext_vector_type(2)));
typedef __bf16 bf16x8 __attribute__((ext_vector_type(8)));
typedef __bf16 bf16x2 __attribute__((ext_vector_type(2)));
typedef short s16x4 __attribute__((ext_vector_type(4)));
typedef unsigned u32x4 __attribute__((ext_vector_type(4)));
typedef unsigned u32x2 __attribute__((ext_vector_type(2)));
#define LAS __attribute__((address_space(3)))
#define DEVI __device__ __forceinline__

constexpr int L = 2048, D = 640, NM = 192, F = 1536;

constexpr u32 WS_HF   = 0;
constexpr u32 WS_W2F  = WS_HF + 32 * 48 * 4 * 1024;
constexpr u32 WS_UT   = WS_W2F + 8 * 48 * 5 * 1024;
constexpr u32 WS_AT   = WS_UT + D * L * 2;
constexpr u32 WS_KIMG = WS_AT + D * L * 2;
constexpr u32 WS_G    = WS_KIMG + D * 2048;
constexpr u32 WS_END  = WS_G + L * 4;

constexpr u32 STG = 28672;
constexpr u32 LDS_RING = 0;
constexpr u32 LDS_RED = 0;
constexpr u32 LDS_ATILE = 61440;
constexpr u32 LDS_CONV = 3 * STG;
constexpr u32 CV_U = 0;
constexpr u32 CV_W5 = 4608;
constexpr u32 CV_W3 = CV_W5 + 3136;
constexpr u32 CV_W9 = CV_W3 + 3136;
constexpr u32 CV_W7 = CV_W9 + 2112;
constexpr u32 CV_KE = CV_W7 + 2112;
constexpr u32 CV_KO = CV_KE + 2048 + 16;
constexpr u32 CV_SZ = CV_KO + 2048;
constexpr u32 CV_TAB = 0;
constexpr u32 LDS_MISC = LDS_CONV + 3 * CV_SZ;
constexpr u32 LDS_SYNC = LDS_MISC + 11264;
constexpr u32 LDS_TOTAL = LDS_SYNC + 64;

struct Params {
  const u16 *u, *Are, *Aim, *Bre, *Bim, *Cre, *Cim, *logdt, *Dskip, *w1, *w2, *gw;
  u16* out;
  char* ws;
};

DEVI float bf2f(u32 x16) { return __uint_as_float(x16 << 16); }
DEVI float bflo(u32 x) { return __uint_as_float(x << 16); }
DEVI float bfhi(u32 x) { return __uint_as_float(x & 0xffff0000u); }
DEVI u32 pk_bf16(float lo, float hi) {
  bf16x2 v = {(__bf16)lo, (__bf16)hi};
  return __builtin_bit_cast(u32, v);
}
DEVI float rbf(float x) { return bflo(pk_bf16(x, x) & 0xffffu); }
DEVI f32x4 mfma16(u32x4 a, u32x4 b, f32x4 c) {
  return __builtin_amdgcn_mfma_f32_16x16x32_bf16(__builtin_bit_cast(bf16x8, a), __builtin_bit_cast(bf16x8, b), c, 0, 0, 0);
}
DEVI u32x4 lds_r128(u32 a) { return *(LAS u32x4*)(uintptr_t)a; }
DEVI u32x2 lds_r64(u32 a) { return *(LAS u32x2*)(uintptr_t)a; }
DEVI u32 lds_r32(u32 a) { return *(LAS u32*)(uintptr_t)a; }
DEVI void lds_w128(u32 a, u32x4 v) { *(LAS u32x4*)(uintptr_t)a = v; }
DEVI void lds_w64(u32 a, u32x2 v) { *(LAS u32x2*)(uintptr_t)a = v; }
DEVI void lds_w32(u32 a, u32 v) { *(LAS u32*)(uintptr_t)a = v; }
DEVI void lds_w16(u32 a, u32 v) { *(LAS u16*)(uintptr_t)a = (u16)v; }
DEVI u32x2 lds_tr(u32 a) { return __builtin_bit_cast(u32x2, __builtin_amdgcn_ds_read_tr16_b64_v4i16((LAS s16x4*)(uintptr_t)a)); }
DEVI u32x4 cat(u32x2 a, u32x2 b) { return u32x4{a.x, a.y, b.x, b.y}; }
typedef __amdgpu_buffer_rsrc_t rsrc_t;
DEVI rsrc_t mkrsrc(const void* p, u32 bytes) { return __builtin_amdgcn_make_buffer_rsrc((void*)p, 0, bytes, 0x00020000); }
template <int AUX> DEVI u32x4 bld128(rsrc_t r, u32 v, u32 s) { return __builtin_amdgcn_raw_buffer_load_b128(r, v, s, AUX); }
template <int AUX> DEVI void bst128(rsrc_t r, u32x4 d, u32 v, u32 s) { __builtin_amdgcn_raw_buffer_store_b128(d, r, v, s, AUX); }
template <int AUX> DEVI void bst64(rsrc_t r, u32x2 d, u32 v, u32 s) { __builtin_amdgcn_raw_buffer_store_b64(d, r, v, s, AUX); }
DEVI u32 rfl(u32 x) { return __builtin_amdgcn_readfirstlane(x); }
DEVI void wait_lgkm0() { asm volatile("s_waitcnt lgkmcnt(0)" ::: "memory"); }
DEVI void wait_vm0() { asm volatile("s_waitcnt vmcnt(0)" ::: "memory"); }
DEVI void dma16(rsrc_t r, u32 voff, u32 soff, u32 ldsdst) {
  asm volatile("s_mov_b32 m0, %0\n\ts_nop 0\n\tbuffer_load_dwordx4 %1, %2, %3 offen lds"
               :: "s"(ldsdst), "v"(voff), "s"(r), "s"(soff) : "memory");
}

DEVI f32x2 cmul(f32x2 a, f32x2 b) {
  f32x2 t, r;
  asm("v_pk_mul_f32 %0, %1, %2 op_sel:[1,1] op_sel_hi:[1,0] neg_lo:[0,1]" : "=v"(t) : "v"(a), "v"(b));
  asm("v_pk_fma_f32 %0, %1, %2, %3 op_sel:[0,0,0] op_sel_hi:[0,1,1]" : "=v"(r) : "v"(a), "v"(b), "v"(t));
  return r;
}
DEVI f32x2 cexp_pow(float ex, float rev, float m) {
  float e = __builtin_amdgcn_exp2f(ex * m);
  float fr = __builtin_amdgcn_fractf(rev * m);
  return f32x2{e * __builtin_amdgcn_cosf(fr), e * __builtin_amdgcn_sinf(fr)};
}
DEVI f32x2 conj(f32x2 a) { return f32x2{a.x, -a.y}; }

template <int AUXST>
DEVI void gemm1_wave(const Params& p, u32 lds0, int w, int lane, int mt, int nt) {
  const rsrc_t rx = mkrsrc(p.u, L * D * 2), rw = mkrsrc(p.w1, D * F * 2);
  const u32 g = lane >> 4, r16 = lane & 15;
  const u32 vx = (u32)(128 * mt + 32 * w + (lane >> 3)) * 1280 + (((lane & 7) ^ (lane >> 3)) & 7) * 16;
  u32 vw[3];
#pragma unroll
  for (int q = 0; q < 3; ++q) {
    u32 o = 1024 * (3 * w + q) + 16 * lane, row = o / 192, pc = (o % 192) >> 4;
    u32 c = pc ^ (2 * ((row >> 3) & 1));
    vw[q] = row * 3072 + 192 * nt + 16 * c;
  }
  auto issue = [&](int c, u32 st) {
#pragma unroll
    for (int q = 0; q < 4; ++q) dma16(rx, vx, c * 128 + q * 10240, lds0 + st + 4096 * w + 1024 * q);
#pragma unroll
    for (int q = 0; q < 3; ++q) dma16(rw, vw[q], c * 196608, lds0 + st + 16384 + 1024 * (3 * w + q));
  };
  const u32 xb0 = lds0 + (32 * w + r16) * 128 + 16 * (g ^ (lane & 7));
  const u32 xb1 = lds0 + (32 * w + r16) * 128 + 16 * ((4 + g) ^ (lane & 7));
  const u32 wrow = lds0 + 16384 + (8 * g + (r16 >> 2)) * 192 + 16 * ((lane & 3) >> 1) + 8 * (lane & 1);
  const u32 wbe = wrow + 32 * (g & 1), wbo = wrow - 32 * (g & 1);
  f32x4 acc[6][2];
#pragma unroll
  for (int a = 0; a < 6; ++a) { acc[a][0] = f32x4{0, 0, 0, 0}; acc[a][1] = f32x4{0, 0, 0, 0}; }
  volatile LAS u32* fl = (volatile LAS u32*)(uintptr_t)(lds0 + LDS_SYNC);
  issue(0, 0); issue(1, STG);
  u32 st = 0;
  for (int c = 0; c < 10; ++c) {
    if (c < 9) asm volatile("s_waitcnt vmcnt(7) lgkmcnt(0)" ::: "memory"); else asm volatile("s_waitcnt vmcnt(0) lgkmcnt(0)" ::: "memory");
    fl[w] = c + 1;
    for (;;) {
      u32x4 f = *(volatile LAS u32x4*)fl;
      u32 m = min(min(f.x, f.y), min(f.z, f.w));
      if (rfl(m) >= (u32)(c + 1)) break;
    }
    asm volatile("" ::: "memory");
    u32 st2 = st + 2 * STG; if (st2 >= 3 * STG) st2 -= 3 * STG;
    if (c < 8) issue(c + 2, st2);
#pragma unroll
    for (int ks = 0; ks < 2; ++ks) {
      u32x4 xb[2], wa[6];
#pragma unroll
      for (int t = 0; t < 2; ++t) xb[t] = lds_r128((ks ? xb1 : xb0) + st + 2048 * t);
#pragma unroll
      for (int a = 0; a < 6; ++a) {
        u32 b = ((a & 1) ? wbo : wbe) + st + 32 * a + ks * 32 * 192;
        wa[a] = cat(lds_tr(b), lds_tr(b + 4 * 192));
      }
#pragma unroll
      for (int a = 0; a < 6; ++a)
#pragma unroll
        for (int t = 0; t < 2; ++t) acc[a][t] = mfma16(wa[a], xb[t], acc[a][t]);
    }
    st += STG; if (st >= 3 * STG) st -= 3 * STG;
  }
  const rsrc_t rh = mkrsrc(p.ws + WS_HF, 32 * 48 * 4 * 1024);
  const int m2 = 2 * mt + (w >> 1);
#pragma unroll
  for (int t = 0; t < 2; ++t)
#pragma unroll
    for (int k = 0; k < 3; ++k) {
      f32x4 a = acc[2 * k][t], b = acc[2 * k + 1][t];
      u32x4 v = {pk_bf16(fmaxf(a[0], 0.f), fmaxf(a[1], 0.f)), pk_bf16(fmaxf(a[2], 0.f), fmaxf(a[3], 0.f)),
                 pk_bf16(fmaxf(b[0], 0.f), fmaxf(b[1], 0.f)), pk_bf16(fmaxf(b[2], 0.f), fmaxf(b[3], 0.f))};
      u32 so = ((m2 * 48 + 3 * nt + k) * 4 + (w & 1) * 2 + t) * 1024;
      bst128<AUXST>(rh, v, lane * 16, so);
    }
}

DEVI void kgen_wave(const Params& p, u32 cv, int lane, int d) {
  const u32 g = lane >> 4, c16 = lane & 15;
  u32 pr[3][6];
  {
    const u16* src[6] = {p.Are, p.Aim, p.Bre, p.Bim, p.Cre, p.Cim};
#pragma unroll
    for (int s = 0; s < 3; ++s)
#pragma unroll
      for (int a = 0; a < 6; ++a) pr[s][a] = src[a][d * NM + 64 * s + lane];
  }
  const float dt = rbf(__builtin_amdgcn_exp2f(bf2f(p.logdt[d]) * 1.44269504088896341f));
  const u32 tw = cv + CV_TAB + 4 * lane;
  const u32 tr = cv + CV_TAB + c16 * 288 + 16 * g;
  f32x4 acc[3] = {f32x4{0, 0, 0, 0}, f32x4{0, 0, 0, 0}, f32x4{0, 0, 0, 0}};
  lds_w32(tw, 0x00003f80u);
#pragma unroll
  for (int s = 0; s < 3; ++s) {
    const float xr = bf2f(pr[s][0]) * dt, xi = bf2f(pr[s][1]) * dt;
    const float br = bf2f(pr[s][2]) * dt, bi = bf2f(pr[s][3]) * dt;
    const float cr = bf2f(pr[s][4]), ci = bf2f(pr[s][5]);
    const float ex = xr * 1.44269504088896341f, rev = xi * 0.15915494309189535f;
    const f32x2 w1 = cexp_pow(ex, rev, 1.f), w8 = cexp_pow(ex, rev, 8.f), w128 = cexp_pow(ex, rev, 128.f);
    {
      const f32x2 w16 = cmul(w8, w8);
      f32x2 e = w16, o = w8;
      lds_w32(tw + 288 * 1, pk_bf16(o.x, o.y));
      lds_w32(tw + 288 * 2, pk_bf16(e.x, e.y));
#pragma unroll
      for (int c = 3; c < 16; ++c) {
        f32x2& v = (c & 1) ? o : e;
        v = cmul(v, w16);
        lds_w32(tw + 288 * c, pk_bf16(v.x, v.y));
      }
    }
    const f32x2 gb = {cr * br - ci * bi, -(cr * bi + ci * br)};
    const f32x2 c1 = conj(w1), c128 = conj(w128);
    const f32x2 c2 = cmul(c1, c1), c4 = cmul(c2, c2);
    const f32x2 c256 = cmul(c128, c128), c512 = cmul(c256, c256), c1024 = cmul(c512, c512);
    u32x4 fb[4];
    {
      f32x2 v = gb;
#pragma unroll
      for (int r = 0; r < 8; ++r) {
        if (r) v = cmul(v, c1);
        f32x2 v2 = cmul(v, c128);
        lds_w32(tw + 288 * (16 + r), pk_bf16(v.x, v.y));
        lds_w32(tw + 288 * (24 + r), pk_bf16(v2.x, v2.y));
      }
#pragma unroll
      for (int k = 0; k < 4; ++k) fb[k] = lds_r128(tr + 64 * k);
#pragma unroll
      for (int k = 0; k < 4; ++k) acc[0] = mfma16(lds_r128(tr + 16 * 288 + 64 * k), fb[k], acc[0]);
    }
    {
      f32x2 v = cmul(gb, c256);
#pragma unroll
      for (int r = 0; r < 4; ++r) {
        if (r) v = cmul(v, c2);
        f32x2 v2 = cmul(v, c128);
        lds_w32(tw + 288 * (16 + r), pk_bf16(v.x, v.y));
        lds_w32(tw + 288 * (20 + r), pk_bf16(v2.x, v2.y));
      }
      f32x2 a = cmul(gb, c512), b = cmul(a, c4);
#pragma unroll
      for (int r = 0; r < 4; ++r) {
        if (r) { a = cmul(a, c128); b = cmul(b, c128); }
        lds_w32(tw + 288 * (24 + 2 * r), pk_bf16(a.x, a.y));
        lds_w32(tw + 288 * (25 + 2 * r), pk_bf16(b.x, b.y));
      }
#pragma unroll
      for (int k = 0; k < 4; ++k) acc[1] = mfma16(lds_r128(tr + 16 * 288 + 64 * k), fb[k], acc[1]);
    }
    {
      f32x2 v = cmul(gb, c1024);
#pragma unroll
      for (int r = 0; r < 9; ++r) {
        if (r) v = cmul(v, c128);
        lds_w32(tw + 288 * (16 + r), pk_bf16(v.x, v.y));
      }
#pragma unroll
      for (int k = 0; k < 4; ++k) acc[2] = mfma16(lds_r128(tr + 16 * 288 + 64 * k), fb[k], acc[2]);
    }
  }
  const u32 ke = cv + CV_KE;
  lds_w128(ke + 16 * lane, u32x4{0, 0, 0, 0});
  lds_w128(ke + 1024 + 16 * lane, u32x4{0, 0, 0, 0});
  const float k512 = rbf(__builtin_bit_cast(float, __builtin_amdgcn_readlane(__builtin_bit_cast(int, acc[1][0]), 32)));
  if (g < 2) {
    const u32 p0 = 256 + 128 * g + 8 * c16;
    float v3 = acc[1][3];
    if (lane == 31) v3 -= k512;
    const u32 a01 = pk_bf16(acc[1][0], acc[1][1]), a23 = pk_bf16(acc[1][2], v3);
    const u32 v0 = a01 & 0xffffu, v1 = a01 >> 16, v2 = a23 & 0xffffu, v3b = a23 >> 16;
    lds_w128(ke + 2 * (544 - p0), u32x4{v3b | (v2 << 16), v2 | (v2 << 16), v1 | (v0 << 16), v0 | (v0 << 16)});
  } else {
#pragma unroll
    for (int r = 0; r < 4; ++r) {
      const u32 pi = 2 * (c16 & 1) + (r & 1), nu = 32 + 16 * (g - 2) + 8 * (r >> 1) + (c16 >> 1);
      lds_w16(ke + 2 * (576 + 64 * pi + 80 - nu), pk_bf16(acc[1][r], 0.f));
    }
  }
  {
    const u32 lb = 128 * (g >> 1) + 4 * (g & 1) + 8 * c16;
#pragma unroll
    for (int r = 0; r < 4; ++r) lds_w16(ke + 2 * (550 - lb - r), pk_bf16(acc[0][r], 0.f));
  }
  if (g < 2) {
#pragma unroll
    for (int r = 0; r < 4; ++r) {
      const u32 pi = c16 & 1, nu = 64 + 32 * g + 8 * r + (c16 >> 1);
      lds_w16(ke + 2 * (832 + 96 * pi + 144 - nu), pk_bf16(acc[2][r], 0.f));
    }
  } else if (lane == 32) {
    lds_w16(ke + 2 * (832 + 144 - 128), pk_bf16(acc[2][0], 0.f));
  }
}

template <int AUXST>
DEVI void misc_wave(const Params& p, u32 lds0, int lane, int b) {
  const int x = b & 7, i = b >> 3, rq = x >> 1, fh = x & 1;
  const u32 ms = lds0 + LDS_MISC;
  const u32 g = lane >> 4, q = (lane & 15) >> 2, pp = lane & 3;
  const rsrc_t rx = mkrsrc(p.u, L * D * 2), rw2 = mkrsrc(p.w2, F * D * 2);
  const int tt = 8 * rq + (i >> 2), dtile = 4 * fh + (i & 3);
  u32x4 tv[10], wv[8], gx[10], gwv[10];
#pragma unroll
  for (int j = 0; j < 10; ++j) {
    u32 id = 64 * j + lane, row = id / 10, c = id % 10;
    tv[j] = bld128<0>(rx, (64 * tt + row) * 1280 + 160 * dtile + 16 * c, 0);
  }
  const int kap = b % 48, c5 = b / 48;
#pragma unroll
  for (int j = 0; j < 8; ++j) wv[j] = bld128<0>(rw2, (32 * kap + 4 * j + g) * 1280 + 256 * c5 + 16 * (lane & 15), 0);
  const int grow = 512 * rq + 256 * fh + 8 * i + (lane >> 3);
  const rsrc_t rg = mkrsrc(p.gw, D * 2);
#pragma unroll
  for (int j = 0; j < 10; ++j) {
    gx[j] = bld128<0>(rx, grow * 1280 + 16 * ((lane & 7) + 8 * j), 0);
    gwv[j] = bld128<0>(rg, 16 * ((lane & 7) + 8 * j), 0);
  }
#pragma unroll
  for (int j = 0; j < 10; ++j) {
    u32 id = 64 * j + lane, row = id / 10, c = id % 10;
    lds_w128(ms + row * 176 + 16 * c, tv[j]);
  }
  const rsrc_t rut = mkrsrc(p.ws + WS_UT, D * L * 2);
#pragma unroll
  for (int dtl = 0; dtl < 5; ++dtl)
#pragma unroll
    for (int hf = 0; hf < 2; ++hf) {
      const u32 t0 = 32 * hf + 8 * g;
      const u32 a = ms + (t0 + q) * 176 + 32 * dtl + 8 * pp;
      u32x4 v = cat(lds_tr(a), lds_tr(a + 4 * 176));
      bst128<AUXST>(rut, v, (80 * dtile + 16 * dtl + (lane & 15)) * 4096 + (64 * tt + t0) * 2, 0);
    }
  if (b < 240) {
#pragma unroll
    for (int j = 0; j < 8; ++j) lds_w128(ms + (4 * j + g) * 288 + 16 * (lane & 15), wv[j]);
    const rsrc_t rwf = mkrsrc(p.ws + WS_W2F, 8 * 48 * 5 * 1024);
#pragma unroll
    for (int T = 0; T < 8; ++T) {
      const u32 a = ms + (4 * g + q) * 288 + 32 * T + 8 * pp;
      u32x4 v = cat(lds_tr(a), lds_tr(a + 16 * 288));
      const int Tg = 8 * c5 + T, j = Tg / 5, nu = Tg % 5;
      bst128<AUXST>(rwf, v, lane * 16, ((j * 48 + kap) * 5 + nu) * 1024);
    }
  }
  float s = 0.f;
#pragma unroll
  for (int j = 0; j < 10; ++j)
#pragma unroll
    for (int e = 0; e < 4; ++e) {
      const u32 a = gx[j][e], w = gwv[j][e];
      const u32 pr = pk_bf16(bflo(a) * bflo(w), bfhi(a) * bfhi(w));
      s += bflo(pr); s += bfhi(pr);
    }
  s += __shfl_xor(s, 1); s += __shfl_xor(s, 2); s += __shfl_xor(s, 4);
  if ((lane & 7) == 0) {
    const float sb = rbf(s);
    const float gv = rbf(1.f / (1.f + __expf(-sb)));
    float* gp = (float*)(p.ws + WS_G);
    if (AUXST) __hip_atomic_store(gp + grow, gv, __ATOMIC_RELAXED, __HIP_MEMORY_SCOPE_AGENT); else gp[grow] = gv;
  }
}

template <int AUX>
DEVI void conv_wave(const Params& p, u32 cv, int lane, int d) {
  const u32 g = lane >> 4, r16 = lane & 15;
  const rsrc_t rut = mkrsrc(p.ws + WS_UT, D * L * 2);
  u32x4 uv[4];
#pragma unroll
  for (int j = 0; j < 4; ++j) uv[j] = bld128<AUX>(rut, d * 4096 + 1024 * j + 16 * lane, 0);
  const u32 dskip = p.Dskip[d];
  const u32 ke = cv + CV_KE, ko = cv + CV_KO;
#pragma unroll
  for (int j = 0; j < 8; ++j) {
    const u32 y = 64 * j + lane;
    const u32 a = lds_r32(ke + 4 * y), b = lds_r32(ke + 4 * y + 4);
    lds_w32(ko + 4 * y, __builtin_amdgcn_alignbit(b, a, 16));
  }
  const u32 ub = cv + CV_U + 512;
  if (lane < 32) lds_w128(cv + CV_U + 16 * lane, u32x4{0, 0, 0, 0});
  if (lane < 16) {
    const u32 arr = lane >> 2;
    const u32 base = arr == 0 ? CV_W5 : arr == 1 ? CV_W3 : arr == 2 ? CV_W9 : CV_W7;
    lds_w128(cv + base + 16 * (lane & 3), u32x4{0, 0, 0, 0});
  }
#pragma unroll
  for (int j = 0; j < 4; ++j) lds_w128(ub + 1024 * j + 16 * lane, uv[j]);
#pragma unroll
  for (int it = 0; it < 3; ++it) {
    const u32 a = ub + 1024 * it + 16 * lane;
    const u32x2 pv = lds_r64(a - 8), nx = lds_r64(a + 16);
    const u32x4 cu = lds_r128(a);
    float v[16];
    v[0] = bflo(pv.x); v[1] = bfhi(pv.x); v[2] = bflo(pv.y); v[3] = bfhi(pv.y);
#pragma unroll
    for (int e = 0; e < 4; ++e) { v[4 + 2 * e] = bflo(cu[e]); v[5 + 2 * e] = bfhi(cu[e]); }
    v[12] = bflo(nx.x); v[13] = bfhi(nx.x); v[14] = bflo(nx.y); v[15] = bfhi(nx.y);
    float w3[8], w5[8], w7[8], w9[8];
#pragma unroll
    for (int e = 0; e < 8; ++e) {
      w3[e] = v[4 + e] + (v[3 + e] + v[5 + e]);
      w5[e] = w3[e] + (v[2 + e] + v[6 + e]);
      if (it < 2) { w7[e] = w5[e] + (v[1 + e] + v[7 + e]); w9[e] = w7[e] + (v[e] + v[8 + e]); }
    }
    const u32 o = 64 + 1024 * it + 16 * lane;
    lds_w128(cv + CV_W5 + o, u32x4{pk_bf16(w5[0], w5[1]), pk_bf16(w5[2], w5[3]), pk_bf16(w5[4], w5[5]), pk_bf16(w5[6], w5[7])});
    lds_w128(cv + CV_W3 + o, u32x4{pk_bf16(w3[0], w3[1]), pk_bf16(w3[2], w3[3]), pk_bf16(w3[4], w3[5]), pk_bf16(w3[6], w3[7])});
    if (it < 2) {
      lds_w128(cv + CV_W9 + o, u32x4{pk_bf16(w9[0], w9[1]), pk_bf16(w9[2], w9[3]), pk_bf16(w9[4], w9[5]), pk_bf16(w9[6], w9[7])});
      lds_w128(cv + CV_W7 + o, u32x4{pk_bf16(w7[0], w7[1]), pk_bf16(w7[2], w7[3]), pk_bf16(w7[4], w7[5]), pk_bf16(w7[6], w7[7])});
    }
  }
  {
    const u32x2 u0 = lds_r64(ub);
    const float a0 = bflo(u0.x), a1 = a0 + bfhi(u0.x), a2 = a1 + bflo(u0.y), a3 = a2 + bfhi(u0.y);
    if (lane == 0) {
      lds_w64(cv + CV_W9 + 64 - 8, u32x2{pk_bf16(a0, a1), pk_bf16(a2, a3)});
      lds_w64(cv + CV_W7 + 64 - 8, u32x2{pk_bf16(0.f, a0), pk_bf16(a1, a2)});
      lds_w64(cv + CV_W5 + 64 - 8, u32x2{0u, pk_bf16(a0, a1)});
      lds_w64(cv + CV_W3 + 64 - 8, u32x2{0u, pk_bf16(0.f, a0)});
    }
  }
  f32x4 acc[8];
#pragma unroll
  for (int t = 0; t < 8; ++t) acc[t] = f32x4{0, 0, 0, 0};
  const u32 kpar = (r16 & 1) ? ko - 2 : ke;
  {
    const u32 ab = kpar + 2 * (550 - 32 * 16 - 16 * (g >> 1) + 8 * (g & 1) - r16);
    u32x4 A[17];
#pragma unroll
    for (int k = 0; k < 17; ++k) {
      const u32 a = ab + 64 * (16 - k);
      A[k] = u32x4{lds_r32(a), lds_r32(a + 4), lds_r32(a + 8), lds_r32(a + 12)};
    }
    const u32 bb = ub + 2 * (16 * r16 - 16 * (g >> 1) + 8 * (g & 1));
#pragma unroll
    for (int z = -7; z <= 56; ++z) {
      const u32x4 B = lds_r128(bb + 64 * z);
#pragma unroll
      for (int t = 0; t < 8; ++t) {
        const int k = 8 * t - z;
        const int kmax = t == 0 ? 7 : (t == 1 ? 15 : 16);
        if (k >= 0 && k <= kmax) acc[t] = mfma16(A[k], B, acc[t]);
      }
    }
    const u32 kc = (*(LAS u16*)(uintptr_t)(ke + 2 * (832 + 144 - 64))) ^ 0x8000u;
    const int idx0 = 1008 + 16 * (g >> 1) - 8 * (g & 1) + r16;
    u32x4 C;
#pragma unroll
    for (int e = 0; e < 4; ++e) {
      const int i0 = idx0 - 2 * e, i1 = i0 - 1;
      const u32 lo = (i0 == 1020 || i0 == 1021) ? kc : 0u, hi = (i1 == 1020 || i1 == 1021) ? kc : 0u;
      C[e] = lo | (hi << 16);
    }
#pragma unroll
    for (int t = 3; t < 8; ++t) {
      const u32x4 B = lds_r128(bb + 2 * (256 * t - 16 * 63));
      acc[t] = mfma16(C, B, acc[t]);
    }
  }
  const u32 zero_a = cv + CV_U + 8 * (lane & 3);
  const u32 q = r16 >> 2, pp = lane & 3;
  {
    u32x4 T[2][3];
#pragma unroll
    for (int pr = 0; pr < 2; ++pr)
#pragma unroll
      for (int dl = 0; dl < 3; ++dl) {
        const u32 a = kpar + 2 * (576 + 64 * (2 * pr + (g >> 1)) + 80 - 16 * (dl + 2) - r16 + 4 * (g & 1));
        T[pr][dl] = u32x4{lds_r32(a), lds_r32(a + 4), lds_r32(a + 16), lds_r32(a + 20)};
      }
    const u32 wb = cv + ((g >> 1) ? CV_W3 - 8 : CV_W5) + 64 + 2 * (16 * (4 * (g & 1) + q) + 4 * pp);
#pragma unroll
    for (int pr = 0; pr < 2; ++pr)
#pragma unroll
      for (int be = -1; be <= 5; ++be) {
        if (be == -1 && pr == 1) continue;
        u32x4 A;
        if (be == -1) {
          const u32 a = ((g & 1) && q == 3) ? wb - 512 + 256 : zero_a;
          const u32x2 hi = lds_tr(a);
          A = u32x4{0u, 0u, hi.x, hi.y};
        } else {
          const u32 a = wb + 512 * be - 16 * pr;
          A = cat(lds_tr(a), lds_tr(a + 256));
        }
#pragma unroll
        for (int dl = 0; dl < 3; ++dl) {
          const int t = be + dl + 2;
          if (t >= 1 && t <= 7) acc[t] = mfma16(A, T[pr][dl], acc[t]);
        }
      }
  }
  {
    u32x4 T[5];
#pragma unroll
    for (int dl = 0; dl < 5; ++dl) {
      const u32 a = kpar + 2 * (832 + 96 * (g >> 1) + 144 - 16 * (dl + 4) - r16 + 4 * (g & 1));
      T[dl] = u32x4{lds_r32(a), lds_r32(a + 4), lds_r32(a + 16), lds_r32(a + 20)};
    }
    const u32 wb = cv + ((g >> 1) ? CV_W7 - 16 : CV_W9) + 64 + 2 * (16 * (4 * (g & 1) + q) + 4 * pp);
#pragma unroll
    for (int be = -1; be <= 3; ++be) {
      u32x4 A;
      if (be == -1) {
        const u32 a = ((g & 1) && q == 3) ? wb - 512 + 256 : zero_a;
        const u32x2 hi = lds_tr(a);
        A = u32x4{0u, 0u, hi.x, hi.y};
      } else {
        const u32 a = wb + 512 * be;
        A = cat(lds_tr(a), lds_tr(a + 256));
      }
#pragma unroll
      for (int dl = 0; dl < 5; ++dl) {
        const int t = be + dl + 4;
        if (t >= 3 && t <= 7) acc[t] = mfma16(A, T[dl], acc[t]);
      }
    }
  }
  const float ds = bf2f(dskip);
  const u32 ob = cv + CV_W5;
#pragma unroll
  for (int t = 0; t < 8; ++t) {
    const u32 tl = 256 * t + 16 * r16 + 4 * g;
    const u32x2 uu = lds_r64(ub + 2 * tl);
    const u32 y01 = pk_bf16(acc[t][0], acc[t][1]), y23 = pk_bf16(acc[t][2], acc[t][3]);
    const u32 s01 = pk_bf16(bflo(uu.x) * ds, bfhi(uu.x) * ds), s23 = pk_bf16(bflo(uu.y) * ds, bfhi(uu.y) * ds);
    lds_w64(ob + 2 * tl, u32x2{pk_bf16(bflo(y01) + bflo(s01), bfhi(y01) + bfhi(s01)), pk_bf16(bflo(y23) + bflo(s23), bfhi(y23) + bfhi(s23))});
  }
  const rsrc_t rat = mkrsrc(p.ws + WS_AT, D * L * 2);
#pragma unroll
  for (int j = 0; j < 4; ++j) bst128<AUX>(rat, lds_r128(ob + 1024 * j + 16 * lane), d * 4096 + 1024 * j + 16 * lane, 0);
}

template <int AUX>
DEVI void gemm2_main(const Params& p, int kw, int lane, int m2, int j, f32x4 (&acc)[4][5]) {
  const rsrc_t rh = mkrsrc(p.ws + WS_HF, 32 * 48 * 4 * 1024), rw = mkrsrc(p.ws + WS_W2F, 8 * 48 * 5 * 1024);
  const u32 vo = lane * 16;
  const u32 hb = (m2 * 48 + 12 * kw) * 4096, wb = (j * 48 + 12 * kw) * 5120;
#pragma unroll
  for (int m = 0; m < 4; ++m)
#pragma unroll
    for (int n = 0; n < 5; ++n) acc[m][n] = f32x4{0, 0, 0, 0};
  constexpr int DP = 3;
  u32x4 hf[DP][4], wf[DP][5];
#pragma unroll
  for (int s = 0; s < DP; ++s) {
#pragma unroll
    for (int n = 0; n < 5; ++n) wf[s][n] = bld128<AUX>(rw, vo + 1024 * (n & 3), wb + s * 5120 + (n >> 2) * 4096);
#pragma unroll
    for (int m = 0; m < 4; ++m) hf[s][m] = bld128<AUX>(rh, vo + 1024 * m, hb + s * 4096);
  }
#pragma unroll
  for (int s = 0; s < 12; ++s) {
    const int sl = s % DP;
#pragma unroll
    for (int m = 0; m < 4; ++m)
#pragma unroll
      for (int n = 0; n < 5; ++n) acc[m][n] = mfma16(wf[sl][n], hf[sl][m], acc[m][n]);
    __builtin_amdgcn_sched_barrier(0);
    if (s + DP < 12) {
#pragma unroll
      for (int n = 0; n < 5; ++n) wf[sl][n] = bld128<AUX>(rw, vo + 1024 * (n & 3), wb + (s + DP) * 5120 + (n >> 2) * 4096);
#pragma unroll
      for (int m = 0; m < 4; ++m) hf[sl][m] = bld128<AUX>(rh, vo + 1024 * m, hb + (s + DP) * 4096);
    }
    __builtin_amdgcn_sched_barrier(0);
  }
}

__global__ __launch_bounds__(512, 2) void phase1_kernel(Params p) {
  extern __shared__ __attribute__((aligned(16))) char smem[];
  const u32 lds0 = (u32)(uintptr_t)(LAS char*)smem;
  const int b = blockIdx.x, lane = threadIdx.x & 63, w = rfl(threadIdx.x >> 6);
  const int x = b & 7, i = b >> 3;
  if (threadIdx.x < 16) lds_w32(lds0 + LDS_SYNC + 4 * threadIdx.x, 0);
  __syncthreads();
  if (w < 4) {
    gemm1_wave<0>(p, lds0, w, lane, 4 * (x >> 1) + (i >> 3), 8 * (x & 1) + (i & 7));
  } else {
    const int xw = w - 4, nch = b < 128 ? 3 : 2, d0 = b < 128 ? 3 * b : 384 + 2 * (b - 128);
    if (xw < nch) {
      const u32 cv = lds0 + LDS_CONV + xw * CV_SZ;
      kgen_wave(p, cv, lane, d0 + xw);
      const rsrc_t rk = mkrsrc(p.ws + WS_KIMG, D * 2048);
#pragma unroll
      for (int j = 0; j < 2; ++j) bst128<0>(rk, lds_r128(cv + CV_KE + 1024 * j + 16 * lane), (d0 + xw) * 2048 + 1024 * j + 16 * lane, 0);
    } else if (xw == 3) {
      misc_wave<0>(p, lds0, lane, b);
    }
  }
}

__global__ __launch_bounds__(512, 2) void phase2_kernel(Params p) {
  extern __shared__ __attribute__((aligned(16))) char smem[];
  const u32 lds0 = (u32)(uintptr_t)(LAS char*)smem;
  const int b = blockIdx.x, lane = threadIdx.x & 63, w = rfl(threadIdx.x >> 6);
  const int x = b & 7, i = b >> 3;
  const int m2 = 4 * x + (i >> 3), j = i & 7;
  if (w >= 4) {
    const int xw = w - 4, nch = b < 128 ? 3 : 2, d0 = b < 128 ? 3 * b : 384 + 2 * (b - 128);
    if (xw < nch) {
      const u32 cv = lds0 + LDS_CONV + xw * CV_SZ;
      const rsrc_t rk = mkrsrc(p.ws + WS_KIMG, D * 2048);
#pragma unroll
      for (int jj = 0; jj < 2; ++jj) lds_w128(cv + CV_KE + 1024 * jj + 16 * lane, bld128<0>(rk, (d0 + xw) * 2048 + 1024 * jj + 16 * lane, 0));
      conv_wave<0>(p, cv, lane, d0 + xw);
    }
  }
}

__global__ __launch_bounds__(512, 2) void phase3_kernel(Params p) {
  extern __shared__ __attribute__((aligned(16))) char smem[];
  const u32 lds0 = (u32)(uintptr_t)(LAS char*)smem;
  const int b = blockIdx.x, lane = threadIdx.x & 63, w = rfl(threadIdx.x >> 6);
  const int x = b & 7, i = b >> 3;
  const int m2 = 4 * x + (i >> 3), j = i & 7;
  const u32 g = lane >> 4, r16 = lane & 15;
  f32x4 acc[4][5];
  if (w < 4) {
    gemm2_main<0>(p, w, lane, m2, j, acc);
#pragma unroll
    for (int m = 0; m < 4; ++m) {
      if (m == w) continue;
      const u32 slot = (w - m - 1) & 3;
#pragma unroll
      for (int n = 0; n < 5; ++n) lds_w128(lds0 + LDS_RED + ((m * 3 + slot) * 5 + n) * 1024 + 16 * lane, __builtin_bit_cast(u32x4, acc[m][n]));
    }
  } else if (w == 7) {
    const rsrc_t rat = mkrsrc(p.ws + WS_AT, D * L * 2);
#pragma unroll
    for (int jj = 0; jj < 10; ++jj) {
      const u32 id = 64 * jj + lane, row = id >> 3, c = id & 7;
      lds_w128(lds0 + LDS_ATILE + row * 160 + 16 * c, bld128<0>(rat, (80 * j + row) * 4096 + 128 * m2 + 16 * c, 0));
    }
  }
  __syncthreads();
  if (w < 4) {
    f32x4 own[5];
#pragma unroll
    for (int m = 0; m < 4; ++m)
      if (m == w) {
#pragma unroll
        for (int n = 0; n < 5; ++n) own[n] = acc[m][n];
      }
    const int t = 64 * m2 + 16 * w + r16;
    const float gv = ((const float*)(p.ws + WS_G))[t];
    const float og = rbf(1.f - gv);
    const rsrc_t ro = mkrsrc(p.out, L * D * 2);
#pragma unroll
    for (int n = 0; n < 5; ++n) {
#pragma unroll
      for (int s = 0; s < 3; ++s) own[n] += __builtin_bit_cast(f32x4, lds_r128(lds0 + LDS_RED + ((w * 3 + s) * 5 + n) * 1024 + 16 * lane));
      const u32x2 av = lds_tr(lds0 + LDS_ATILE + (16 * n + 4 * g + (r16 >> 2)) * 160 + 32 * w + 8 * (lane & 3));
      const u32 b01 = pk_bf16(own[n][0], own[n][1]), b23 = pk_bf16(own[n][2], own[n][3]);
      const u32 ga01 = pk_bf16(gv * bflo(av.x), gv * bfhi(av.x)), ga23 = pk_bf16(gv * bflo(av.y), gv * bfhi(av.y));
      const u32 gb01 = pk_bf16(og * bflo(b01), og * bfhi(b01)), gb23 = pk_bf16(og * bflo(b23), og * bfhi(b23));
      const u32x2 o = {pk_bf16(bflo(ga01) + bflo(gb01), bfhi(ga01) + bfhi(gb01)), pk_bf16(bflo(ga23) + bflo(gb23), bfhi(ga23) + bfhi(gb23))};
      bst64<0>(ro, o, t * 1280 + (80 * j + 16 * n + 4 * g) * 2, 0);
    }
  }
}

extern "C" void kernel_launch(void* const* d_in, const int* in_sizes, int n_in, void* d_out, int out_size, void* d_ws, size_t ws_size,
                              hipStream_t stream) {
  Params p;
  p.u = (const u16*)d_in[0]; p.Are = (const u16*)d_in[1]; p.Aim = (const u16*)d_in[2]; p.Bre = (const u16*)d_in[3];
  p.Bim = (const u16*)d_in[4]; p.Cre = (const u16*)d_in[5]; p.Cim = (const u16*)d_in[6]; p.logdt = (const u16*)d_in[7];
  p.Dskip = (const u16*)d_in[8]; p.w1 = (const u16*)d_in[10]; p.w2 = (const u16*)d_in[11]; p.gw = (const u16*)d_in[12];
  p.out = (u16*)d_out; p.ws = (char*)d_ws;
  (void)hipFuncSetAttribute((const void*)phase1_kernel, hipFuncAttributeMaxDynamicSharedMemorySize, LDS_TOTAL);
  (void)hipFuncSetAttribute((const void*)phase2_kernel, hipFuncAttributeMaxDynamicSharedMemorySize, LDS_TOTAL);
  (void)hipFuncSetAttribute((const void*)phase3_kernel, hipFuncAttributeMaxDynamicSharedMemorySize, LDS_TOTAL);
  phase1_kernel<<<256, 512, LDS_TOTAL, stream>>>(p);
  phase2_kernel<<<256, 512, LDS_TOTAL, stream>>>(p);
  phase3_kernel<<<256, 512, LDS_TOTAL, stream>>>(p);
}
```

```cpp
#include <hip/hip_runtime.h>
#include <stdint.h>

typedef unsigned short u16;
typedef unsigned u32;
typedef unsigned long long u64;
typedef float f32x4 __attribute__((ext_vector_type(4)));
typedef float f32x2 __attribute__((ext_vector_type(2)));
typedef __bf16 bf16x8 __attribute__((ext_vector_type(8)));
typedef __bf16 bf16x2 __attribute__((ext_vector_type(2)));
typedef short s16x4 __attribute__((ext_vector_type(4)));
typedef unsigned u32x4 __attribute__((ext_vector_type(4)));
typedef unsigned u32x2 __attribute__((ext_vector_type(2)));
#define LAS __attribute__((address_space(3)))
#define GAS __attribute__((address_space(1)))
#define DEVI __device__ __forceinline__
#define SB() __builtin_amdgcn_sched_barrier(0)
#define RLX_AGENT __ATOMIC_RELAXED, __HIP_MEMORY_SCOPE_AGENT

constexpr int L = 2048, D = 640, NM = 192, F = 1536;
constexpr int SC1 = 16;

constexpr u32 WS_HF   = 0;
constexpr u32 WS_W2F  = WS_HF + 32 * 48 * 4 * 1024;
constexpr u32 WS_UT   = WS_W2F + 8 * 48 * 5 * 1024;
constexpr u32 WS_AT   = WS_UT + D * L * 2;
constexpr u32 WS_KIMG = WS_AT + D * L * 2;
constexpr u32 WS_G    = WS_KIMG + D * 2048;
constexpr u32 WS_EP   = WS_G + L * 4;
constexpr u32 WS_FU   = WS_EP + 256;
constexpr u32 WS_FW   = WS_FU + 8 * 32 * 8;
constexpr u32 WS_FG   = WS_FW + 5 * 64 * 8;
constexpr u32 WS_FH   = WS_FG + 32 * 8 * 8;
constexpr u32 WS_FA   = WS_FH + 32 * 32 * 8;
constexpr u32 WS_END  = WS_FA + 640 * 8;

constexpr u32 STG = 28672;
constexpr u32 LDS_RED = 0;
constexpr u32 LDS_ATILE = 61440;
constexpr u32 LDS_GT = LDS_ATILE + 12800;
constexpr u32 LDS_CONV = 3 * STG;
constexpr u32 CV_U = 0;
constexpr u32 CV_W5 = 4608;
constexpr u32 CV_W3 = CV_W5 + 3136;
constexpr u32 CV_W9 = CV_W3 + 3136;
constexpr u32 CV_W7 = CV_W9 + 2112;
constexpr u32 CV_KE = CV_W7 + 2112;
constexpr u32 CV_KO = CV_KE + 2048 + 16;
constexpr u32 CV_SZ = CV_KO + 2048;
constexpr u32 CV_TAB = 0;
constexpr u32 LDS_SYNC = LDS_CONV + 3 * CV_SZ;
constexpr u32 SY_DONE = 0  , SY_FULL = 4, SY_AUX = 5, SY_H = 6, SY_MISC = 7, SY_TAG = 8  , SY_TAGV = 10;
constexpr u32 LDS_DBG = LDS_SYNC + 64;
constexpr u32 LDS_TOTAL = LDS_SYNC + 64 + 1024;
constexpr u32 SPIN_CAP = 1u << 20, POLL_CAP = 1u << 16;

struct Params {
  const u16 *u, *Are, *Aim, *Bre, *Bim, *Cre, *Cim, *logdt, *Dskip, *w1, *w2, *gw;
  u16* out;
  char* ws;
};

#define STAMP(k) do {} while (0)
#define STAMP_ARG
#define TRACE(i) do {} while (0)
#define STAMP_PASS

DEVI float bf2f(u32 x16) { return __uint_as_float(x16 << 16); }
DEVI float bflo(u32 x) { return __uint_as_float(x << 16); }
DEVI float bfhi(u32 x) { return __uint_as_float(x & 0xffff0000u); }
DEVI u32 pk_bf16(float lo, float hi) {
  bf16x2 v = {(__bf16)lo, (__bf16)hi};
  return __builtin_bit_cast(u32, v);
}
DEVI float rbf(float x) { return bflo(pk_bf16(x, x) & 0xffffu); }
DEVI float dot2bf(u32 a, u32 b, float c) { return __builtin_amdgcn_fdot2_f32_bf16(__builtin_bit_cast(bf16x2, a), __builtin_bit_cast(bf16x2, b), c, false); }
DEVI f32x4 mfma16(u32x4 a, u32x4 b, f32x4 c) {
  return __builtin_amdgcn_mfma_f32_16x16x32_bf16(__builtin_bit_cast(bf16x8, a), __builtin_bit_cast(bf16x8, b), c, 0, 0, 0);
}
DEVI u32x4 lds_r128(u32 a) { return *(LAS u32x4*)(uintptr_t)a; }
DEVI u32x2 lds_r64(u32 a) { return *(LAS u32x2*)(uintptr_t)a; }
DEVI u32 lds_r32(u32 a) { return *(LAS u32*)(uintptr_t)a; }
DEVI void lds_w128(u32 a, u32x4 v) { *(LAS u32x4*)(uintptr_t)a = v; }
DEVI void lds_w64(u32 a, u32x2 v) { *(LAS u32x2*)(uintptr_t)a = v; }
DEVI void lds_w32(u32 a, u32 v) { *(LAS u32*)(uintptr_t)a = v; }
DEVI void lds_w16(u32 a, u32 v) { *(LAS u16*)(uintptr_t)a = (u16)v; }
DEVI u32 lds_vr32(u32 a) { return *(volatile LAS u32*)(uintptr_t)a; }
DEVI u32x4 lds_vr128(u32 a) { return *(volatile LAS u32x4*)(uintptr_t)a; }
DEVI void lds_vw32(u32 a, u32 v) { *(volatile LAS u32*)(uintptr_t)a = v; }
DEVI u32x2 lds_tr(u32 a) { return __builtin_bit_cast(u32x2, __builtin_amdgcn_ds_read_tr16_b64_v4i16((LAS s16x4*)(uintptr_t)a)); }
DEVI u32x4 cat(u32x2 a, u32x2 b) { return u32x4{a.x, a.y, b.x, b.y}; }
typedef __amdgpu_buffer_rsrc_t rsrc_t;
DEVI rsrc_t mkrsrc(const void* p, u32 bytes) { return __builtin_amdgcn_make_buffer_rsrc((void*)p, 0, bytes, 0x00020000); }
template <int AUX> DEVI u32x4 bld128(rsrc_t r, u32 v, u32 s) { return __builtin_amdgcn_raw_buffer_load_b128(r, v, s, AUX); }
template <int AUX> DEVI void bst128(rsrc_t r, u32x4 d, u32 v, u32 s) { __builtin_amdgcn_raw_buffer_store_b128(d, r, v + s, 0, AUX); }
template <int AUX> DEVI void bst64(rsrc_t r, u32x2 d, u32 v, u32 s) { __builtin_amdgcn_raw_buffer_store_b64(d, r, v + s, 0, AUX); }
DEVI u32 rfl(u32 x) { return __builtin_amdgcn_readfirstlane(x); }
DEVI void wait_lgkm0() { asm volatile("s_waitcnt lgkmcnt(0)" ::: "memory"); }
DEVI void wait_vm0() { asm volatile("s_waitcnt vmcnt(0)" ::: "memory"); }
DEVI void fence_c() { asm volatile("" ::: "memory"); }
DEVI u32 min4(u32x4 f) { return min(min(f.x, f.y), min(f.z, f.w)); }
DEVI void sy_set(u32 a, u32 v, int lane) { if (lane == 0) lds_vw32(a, v); }
DEVI void sy_wait_ge(u32 a, u32 v) { u32 n = 0; while (rfl(lds_vr32(a)) < v && ++n < SPIN_CAP) {} fence_c(); }
DEVI u64 gld64(const char* ws, u32 off) { return __hip_atomic_load((GAS u64*)(ws + off), RLX_AGENT); }
DEVI void gst64(char* ws, u32 off, u64 v) { __hip_atomic_store((GAS u64*)(ws + off), v, RLX_AGENT); }
DEVI u64 get_tag(u32 lds0) {
  sy_wait_ge(lds0 + LDS_SYNC + 4 * SY_TAGV, 1);
  const u32x2 t = *(volatile LAS u32x2*)(uintptr_t)(lds0 + LDS_SYNC + 4 * SY_TAG);
  return ((u64)rfl(t.y) << 32) | rfl(t.x);
}
DEVI void poll_flags(const char* ws, u32 off, bool act, u64 tag) {
  for (u32 n = 0; n < POLL_CAP; ++n) {
    const u64 v = act ? gld64(ws, off) : tag;
    if (__all(v == tag)) break;
    __builtin_amdgcn_s_sleep(4);
  }
  fence_c();
}

DEVI void cmul2(f32x2& r0, f32x2 a0, f32x2 b0, f32x2& r1, f32x2 a1, f32x2 b1) {
  f32x2 t0, t1;
  asm("v_pk_mul_f32 %2, %4, %5 op_sel:[1,1] op_sel_hi:[1,0] neg_lo:[0,1]\n\t"
      "v_pk_mul_f32 %3, %6, %7 op_sel:[1,1] op_sel_hi:[1,0] neg_lo:[0,1]\n\t"
      "v_pk_fma_f32 %0, %4, %5, %2 op_sel:[0,0,0] op_sel_hi:[0,1,1]\n\t"
      "v_pk_fma_f32 %1, %6, %7, %3 op_sel:[0,0,0] op_sel_hi:[0,1,1]"
      : "=&v"(r0), "=&v"(r1), "=&v"(t0), "=&v"(t1) : "v"(a0), "v"(b0), "v"(a1), "v"(b1));
}
DEVI f32x2 cexp_pow(float ex, float rev, float m) {
  float e = __builtin_amdgcn_exp2f(ex * m);
  float fr = __builtin_amdgcn_fractf(rev * m);
  return f32x2{e * __builtin_amdgcn_cosf(fr), e * __builtin_amdgcn_sinf(fr)};
}
DEVI f32x2 conj(f32x2 a) { return f32x2{a.x, -a.y}; }
DEVI u32 pk2(f32x2 v) { return pk_bf16(v.x, v.y); }

struct T_ { static constexpr bool value = true; };
struct F_ { static constexpr bool value = false; };

DEVI void misc_job(const Params& p, u32 lds0, int w, int lane, int b) {
  const int x = b & 7, i = b >> 3, rq = x >> 1, fh = x & 1;
  const u32 g = lane >> 4, q = (lane & 15) >> 2, pp = lane & 3;
  const rsrc_t rx = mkrsrc(p.u, L * D * 2);
  if (w < 2) {
    const u32 ms = lds0 + 2 * STG + 5632 * w;
    const int tt = 8 * rq + (i >> 2), dtile = 4 * fh + (i & 3);
    u32x4 tv[5];
#pragma unroll
    for (int j = 0; j < 5; ++j) {
      const u32 id = 64 * j + lane, row = id / 10, c = id % 10;
      tv[j] = bld128<0>(rx, (64 * tt + 32 * w + row) * 1280 + 160 * dtile + 16 * c, 0);
    }
#pragma unroll
    for (int j = 0; j < 5; ++j) {
      const u32 id = 64 * j + lane, row = id / 10, c = id % 10;
      lds_w128(ms + row * 176 + 16 * c, tv[j]);
    }
    fence_c();
    const rsrc_t rut = mkrsrc(p.ws + WS_UT, D * L * 2);
#pragma unroll
    for (int dtl = 0; dtl < 5; ++dtl) {
      const u32 a = ms + (8 * g + q) * 176 + 32 * dtl + 8 * pp;
      const u32x4 v = cat(lds_tr(a), lds_tr(a + 4 * 176));
      bst128<SC1>(rut, v, (80 * dtile + 16 * dtl + (lane & 15)) * 4096 + (64 * tt + 32 * w + 8 * g) * 2, 0);
    }
  } else if (w == 2) {
    if (b < 240) {
      const u32 ms = lds0 + 2 * STG + 11264;
      const rsrc_t rw2 = mkrsrc(p.w2, F * D * 2);
      const int kap = b % 48, c5 = b / 48;
      u32x4 wv[8];
#pragma unroll
      for (int j = 0; j < 8; ++j) wv[j] = bld128<0>(rw2, (32 * kap + 4 * j + g) * 1280 + 256 * c5 + 16 * (lane & 15), 0);
#pragma unroll
      for (int j = 0; j < 8; ++j) lds_w128(ms + (4 * j + g) * 288 + 16 * (lane & 15), wv[j]);
      fence_c();
      const rsrc_t rwf = mkrsrc(p.ws + WS_W2F, 8 * 48 * 5 * 1024);
#pragma unroll
      for (int T = 0; T < 8; ++T) {
        const u32 a = ms + (4 * g + q) * 288 + 32 * T + 8 * pp;
        const u32x4 v = cat(lds_tr(a), lds_tr(a + 16 * 288));
        const int Tg = 8 * c5 + T, j = Tg / 5, nu = Tg % 5;
        bst128<SC1>(rwf, v, lane * 16, ((j * 48 + kap) * 5 + nu) * 1024);
      }
    }
  } else {
    const int grow = 512 * rq + 256 * fh + 8 * i + (lane >> 3);
    const rsrc_t rg = mkrsrc(p.gw, D * 2);
    u32x4 gx[10], gwv[10];
#pragma unroll
    for (int j = 0; j < 10; ++j) {
      gx[j] = bld128<0>(rx, grow * 1280 + 16 * ((lane & 7) + 8 * j), 0);
      gwv[j] = bld128<0>(rg, 16 * ((lane & 7) + 8 * j), 0);
    }
    float s0 = 0.f, s1 = 0.f;
#pragma unroll
    for (int j = 0; j < 10; ++j)
#pragma unroll
      for (int e = 0; e < 4; e += 2) {
        s0 = dot2bf(gx[j][e], gwv[j][e], s0);
        s1 = dot2bf(gx[j][e + 1], gwv[j][e + 1], s1);
      }
    float s = s0 + s1;
    s += __shfl_xor(s, 1); s += __shfl_xor(s, 2); s += __shfl_xor(s, 4);
    if ((lane & 7) == 0) {
      const float sb = rbf(s);
      const float gv = rbf(1.f / (1.f + __expf(-sb)));
      __hip_atomic_store((GAS float*)(p.ws + WS_G) + grow, gv, RLX_AGENT);
    }
  }
}

DEVI void gemm1_loader(const Params& p, u32 lds0, int lane, int mt, int nt, int rot STAMP_ARG) {
  const rsrc_t rx = mkrsrc(p.u, L * D * 2), rw = mkrsrc(p.w1, D * F * 2);
  const u32 vx = (u32)(128 * mt + (lane >> 3)) * 1280 + (((lane & 7) ^ (lane >> 3)) & 7) * 16;
  u32 vw[3];
#pragma unroll
  for (int q = 0; q < 3; ++q) {
    const u32 o = 1024 * q + 16 * lane, row = o / 192, pc = (o % 192) >> 4;
    vw[q] = row * 3072 + 192 * nt + 16 * (pc ^ (2 * ((row >> 3) & 1)));
  }
  const u32 sy = lds0 + LDS_SYNC;
  u32 st = 0;
  int cc = rot;
  for (int k = 0; k < 10; ++k) {
    TRACE(4 * k);
    if (k >= 2) {
      u32 n = 0;
      while (rfl(min4(lds_vr128(sy + 4 * SY_DONE))) < (u32)(k - 1) && ++n < SPIN_CAP) {}
      fence_c();
    }
    TRACE(4 * k + 1);
    u32 m = lds0 + st, sx = cc * 128, sw = cc * 196608;
    asm volatile(
        "s_mov_b32 m0, %[m]\n\t"
        ".rept 16\n\t"
        "s_nop 0\n\t"
        "buffer_load_dwordx4 %[vx], %[rx], %[sx] offen lds\n\t"
        "s_add_u32 m0, m0, 0x400\n\t"
        "s_add_u32 %[sx], %[sx], 10240\n\t"
        ".endr\n\t"
        ".rept 4\n\t"
        "buffer_load_dwordx4 %[v0], %[rw], %[sw] offen lds\n\t"
        "s_add_u32 m0, m0, 0x400\n\t"
        "s_nop 0\n\t"
        "buffer_load_dwordx4 %[v1], %[rw], %[sw] offen lds\n\t"
        "s_add_u32 m0, m0, 0x400\n\t"
        "s_nop 0\n\t"
        "buffer_load_dwordx4 %[v2], %[rw], %[sw] offen lds\n\t"
        "s_add_u32 m0, m0, 0x400\n\t"
        "s_add_u32 %[sw], %[sw], 49152\n\t"
        ".endr"
        : [sx] "+s"(sx), [sw] "+s"(sw)
        : [m] "s"(m), [vx] "v"(vx), [v0] "v"(vw[0]), [v1] "v"(vw[1]), [v2] "v"(vw[2]), [rx] "s"(rx), [rw] "s"(rw)
        : "memory", "scc");
    TRACE(4 * k + 2);
    if (k >= 1) {
      asm volatile("s_waitcnt vmcnt(28)" ::: "memory");
      sy_set(sy + 4 * SY_FULL, k, lane);
    }
    TRACE(4 * k + 3);
    st = st == 2 * STG ? 0 : st + STG;
    cc = cc == 9 ? 0 : cc + 1;
  }
  wait_vm0();
  sy_set(sy + 4 * SY_FULL, 10, lane);
}

DEVI void gemm1_wave(const Params& p, u32 lds0, int w, int lane, int mt, int nt, int b STAMP_ARG) {
  const u32 g = lane >> 4, r16 = lane & 15;
  const u32 sy = lds0 + LDS_SYNC;
  const u32 xb0 = lds0 + (32 * w + r16) * 128 + 16 * (g ^ (lane & 7));
  const u32 xb1 = lds0 + (32 * w + r16) * 128 + 16 * ((4 + g) ^ (lane & 7));
  const u32 wrow = lds0 + 16384 + (8 * g + (r16 >> 2)) * 192 + 16 * ((lane & 3) >> 1) + 8 * (lane & 1);
  const u32 wbe = wrow + 32 * (g & 1), wbo = wrow - 32 * (g & 1);
  f32x4 acc[6][2];
#pragma unroll
  for (int a = 0; a < 6; ++a) { acc[a][0] = f32x4{0, 0, 0, 0}; acc[a][1] = f32x4{0, 0, 0, 0}; }
  u32x4 xa[2], wa[6], xb[2], wb[6];
  auto rd = [&](int j, u32 st, int ks, u32x4 (&X)[2], u32x4 (&W)[6]) {
    if (j < 2) X[j] = lds_r128((ks ? xb1 : xb0) + st + 2048 * j);
    else {
      const int a = (j - 2) >> 1, h = (j - 2) & 1;
      const u32x2 v = lds_tr(((a & 1) ? wbo : wbe) + st + 32 * a + ks * 32 * 192 + h * 4 * 192);
      if (h == 0) { W[a].x = v.x; W[a].y = v.y; } else { W[a].z = v.x; W[a].w = v.y; }
    }
  };
  auto chunk = [&](int c, u32 st, u32 stn, auto LAST) {
    constexpr bool last = decltype(LAST)::value;
#pragma unroll
    for (int m = 0; m < 12; ++m) {
      acc[m >> 1][m & 1] = mfma16(wa[m >> 1], xa[m & 1], acc[m >> 1][m & 1]);
      rd(m, st, 1, xb, wb);
      if (m >= 10) rd(m + 2, st, 1, xb, wb);
      SB();
    }
    wait_lgkm0();
    u32 f = 0;
    if (!last) { sy_set(sy + 4 * SY_DONE + 4 * w, c + 2, lane); f = lds_vr32(sy + 4 * SY_FULL); }
    SB();
#pragma unroll
    for (int m = 0; m < 4; ++m) acc[m >> 1][m & 1] = mfma16(wb[m >> 1], xb[m & 1], acc[m >> 1][m & 1]);
    SB();
    if (!last) {
      u32 n = 0;
      if (w == 0) TRACE(64 + 2 * c);
      while (rfl(f) < (u32)(c + 2) && ++n < SPIN_CAP) f = lds_vr32(sy + 4 * SY_FULL);
      fence_c();
      if (w == 0) TRACE(64 + 2 * c + 1);
    }
#pragma unroll
    for (int m = 4; m < 12; ++m) {
      if (!last && m < 11) { rd(2 * (m - 4), stn, 0, xa, wa); rd(2 * (m - 4) + 1, stn, 0, xa, wa); }
      acc[m >> 1][m & 1] = mfma16(wb[m >> 1], xb[m & 1], acc[m >> 1][m & 1]);
      SB();
    }
  };
  sy_wait_ge(sy + 4 * SY_FULL, 1);
  STAMP(2);
#pragma unroll
  for (int j = 0; j < 14; ++j) rd(j, 0, 0, xa, wa);
  u32 st = 0;
  for (int c = 0; c < 9; ++c) {
    const u32 stn = st == 2 * STG ? 0 : st + STG;
    chunk(c, st, stn, F_{});
    st = stn;
    if (c == 2) {
      wait_vm0();
      u32 old = 0;
      if (lane == 0) old = __hip_atomic_fetch_add((LAS u32*)(uintptr_t)(sy + 4 * SY_MISC), 1u, __ATOMIC_RELAXED, __HIP_MEMORY_SCOPE_WORKGROUP);
      if (rfl(old) == 3) {
        const u64 tag = get_tag(lds0);
        const int x = b & 7, i = b >> 3;
        const int tt = 8 * (x >> 1) + (i >> 2), dtile = 4 * (x & 1) + (i & 3);
        const int grow8 = 64 * (x >> 1) + 32 * (x & 1) + i;
        const u32 off = lane == 0 ? WS_FU + 8 * (dtile * 32 + tt) : lane == 1 ? WS_FG + 8 * grow8 : WS_FW + 8 * ((b / 48) * 64 + b % 48);
        if (lane < (b < 240 ? 3 : 2)) gst64(p.ws, off, tag);
      }
    }
    if (c == 4) STAMP(3);
  }
  chunk(9, st, 0, T_{});
  STAMP(4);
  const rsrc_t rh = mkrsrc(p.ws + WS_HF, 32 * 48 * 4 * 1024);
  const int m2 = 2 * mt + (w >> 1);
#pragma unroll
  for (int t = 0; t < 2; ++t)
#pragma unroll
    for (int k = 0; k < 3; ++k) {
      const f32x4 a = acc[2 * k][t], c = acc[2 * k + 1][t];
      const u32x4 v = {pk_bf16(fmaxf(a[0], 0.f), fmaxf(a[1], 0.f)), pk_bf16(fmaxf(a[2], 0.f), fmaxf(a[3], 0.f)),
                       pk_bf16(fmaxf(c[0], 0.f), fmaxf(c[1], 0.f)), pk_bf16(fmaxf(c[2], 0.f), fmaxf(c[3], 0.f))};
      bst128<SC1>(rh, v, lane * 16, ((m2 * 48 + 3 * nt + k) * 4 + (w & 1) * 2 + t) * 1024);
    }
  const u64 tag = get_tag(lds0);
  wait_vm0();
  if (lane == 0) gst64(p.ws, WS_FH + 8 * ((m2 * 16 + nt) * 2 + (w & 1)), tag);
}

DEVI void kgen_wave(const Params& p, u32 lds0, u32 cv, int lane, int d, bool first STAMP_ARG) {
  const u32 g = lane >> 4, c16 = lane & 15;
  u32 pr[3][6];
  {
    const u16* src[6] = {p.Are, p.Aim, p.Bre, p.Bim, p.Cre, p.Cim};
#pragma unroll
    for (int s = 0; s < 3; ++s)
#pragma unroll
      for (int a = 0; a < 6; ++a) pr[s][a] = src[a][d * NM + 64 * s + lane];
  }
  const float dt = rbf(__builtin_amdgcn_exp2f(bf2f(p.logdt[d]) * 1.44269504088896341f));
  if (first) {
    const u64 tag = *(const GAS u64*)(p.ws + WS_EP) + 1;
    if (lane == 0) { lds_vw32(lds0 + LDS_SYNC + 4 * SY_TAG, (u32)tag); lds_vw32(lds0 + LDS_SYNC + 4 * SY_TAG + 4, (u32)(tag >> 32)); lds_vw32(lds0 + LDS_SYNC + 4 * SY_TAGV, 1); }
  }
  const u32 tw = cv + CV_TAB + 4 * lane;
  const u32 tr = cv + CV_TAB + c16 * 288 + 16 * g;
  f32x4 acc[3] = {f32x4{0, 0, 0, 0}, f32x4{0, 0, 0, 0}, f32x4{0, 0, 0, 0}};
  lds_w32(tw, 0x00003f80u);
  STAMP(1);
#pragma unroll 1
  for (int s = 0; s < 3; ++s) {
    const u32 a0 = s == 0 ? pr[0][0] : s == 1 ? pr[1][0] : pr[2][0], a1 = s == 0 ? pr[0][1] : s == 1 ? pr[1][1] : pr[2][1];
    const u32 a2 = s == 0 ? pr[0][2] : s == 1 ? pr[1][2] : pr[2][2], a3 = s == 0 ? pr[0][3] : s == 1 ? pr[1][3] : pr[2][3];
    const u32 a4 = s == 0 ? pr[0][4] : s == 1 ? pr[1][4] : pr[2][4], a5 = s == 0 ? pr[0][5] : s == 1 ? pr[1][5] : pr[2][5];
    const float xr = bf2f(a0) * dt, xi = bf2f(a1) * dt, br = bf2f(a2) * dt, bi = bf2f(a3) * dt, cr = bf2f(a4), ci = bf2f(a5);
    const float ex = xr * 1.44269504088896341f, rev = xi * 0.15915494309189535f;
    const f32x2 w1 = cexp_pow(ex, rev, 1.f), w8 = cexp_pow(ex, rev, 8.f), w128 = cexp_pow(ex, rev, 128.f);
    const f32x2 gb = {cr * br - ci * bi, -(cr * bi + ci * br)};
    const f32x2 c1 = conj(w1), c128 = conj(w128);
    f32x2 c2, c4, w16, c256, c512, c1024;
    cmul2(c2, c1, c1, c256, c128, c128);
    cmul2(w16, w8, w8, c512, c256, c256);
    cmul2(c4, c2, c2, c1024, c512, c512);
    {
      f32x2 o = w8, e = w16, v = gb, v2;
      lds_w32(tw + 288 * 1, pk2(o));
      lds_w32(tw + 288 * 2, pk2(e));
      lds_w32(tw + 288 * 16, pk2(v));
#pragma unroll
      for (int r = 0; r < 7; ++r) {
        f32x2 on, vn;
        cmul2(on, o, w16, v2, v, c128);
        lds_w32(tw + 288 * (3 + 2 * r), pk2(on));
        lds_w32(tw + 288 * (24 + r), pk2(v2));
        if (r < 6) {
          f32x2 en;
          cmul2(en, e, w16, vn, v, c1);
          lds_w32(tw + 288 * (4 + 2 * r), pk2(en));
          e = en;
        } else {
          f32x2 dummy;
          cmul2(dummy, v, c1, vn, v, c1);
        }
        lds_w32(tw + 288 * (17 + r), pk2(vn));
        o = on; v = vn;
      }
      f32x2 a, bq;
      cmul2(v2, v, c128, a, gb, c256);
      lds_w32(tw + 288 * 31, pk2(v2));
      fence_c();
      u32x4 fb[4];
#pragma unroll
      for (int k = 0; k < 4; ++k) fb[k] = lds_r128(tr + 64 * k);
      u32x4 fa[4];
#pragma unroll
      for (int k = 0; k < 4; ++k) fa[k] = lds_r128(tr + 16 * 288 + 64 * k);
      fence_c();
      cmul2(bq, gb, c512, v, gb, c1024);
      f32x2 t1[16];
      t1[0] = a; t1[8] = bq;
      cmul2(t1[1], a, c2, t1[9], bq, c4);
      cmul2(t1[2], t1[1], c2, t1[4], a, c128);
      cmul2(t1[3], t1[2], c2, t1[5], t1[1], c128);
      cmul2(t1[6], t1[2], c128, t1[7], t1[3], c128);
      cmul2(t1[10], t1[8], c128, t1[11], t1[9], c128);
      cmul2(t1[12], t1[10], c128, t1[13], t1[11], c128);
      cmul2(t1[14], t1[12], c128, t1[15], t1[13], c128);
#pragma unroll
      for (int k = 0; k < 4; ++k) acc[0] = mfma16(fa[k], fb[k], acc[0]);
#pragma unroll
      for (int r = 0; r < 16; ++r) lds_w32(tw + 288 * (16 + r), pk2(t1[r]));
      fence_c();
#pragma unroll
      for (int k = 0; k < 4; ++k) fa[k] = lds_r128(tr + 16 * 288 + 64 * k);
      fence_c();
      f32x2 t2[9];
      t2[0] = v;
      f32x2 dm;
      cmul2(t2[1], v, c128, t2[2], v, c256);
      cmul2(t2[3], t2[1], c256, t2[4], t2[2], c256);
      cmul2(t2[5], t2[3], c256, t2[6], t2[4], c256);
      cmul2(t2[7], t2[5], c256, t2[8], t2[6], c256);
#pragma unroll
      for (int k = 0; k < 4; ++k) acc[1] = mfma16(fa[k], fb[k], acc[1]);
#pragma unroll
      for (int r = 0; r < 9; ++r) lds_w32(tw + 288 * (16 + r), pk2(t2[r]));
      fence_c();
#pragma unroll
      for (int k = 0; k < 4; ++k) fa[k] = lds_r128(tr + 16 * 288 + 64 * k);
      fence_c();
#pragma unroll
      for (int k = 0; k < 4; ++k) acc[2] = mfma16(fa[k], fb[k], acc[2]);
      (void)dm;
    }
  }
  STAMP(4);
  const u32 ke = cv + CV_KE;
  lds_w128(ke + 16 * lane, u32x4{0, 0, 0, 0});
  lds_w128(ke + 1024 + 16 * lane, u32x4{0, 0, 0, 0});
  fence_c();
  const float k512 = rbf(__int_as_float(__builtin_amdgcn_readlane(__float_as_int(acc[1][0]), 32)));
  if (g < 2) {
    const u32 p0 = 256 + 128 * g + 8 * c16;
    float v3 = acc[1][3];
    if (lane == 31) v3 -= k512;
    const u32 a01 = pk_bf16(acc[1][0], acc[1][1]), a23 = pk_bf16(acc[1][2], v3);
    const u32 v0 = a01 & 0xffffu, v1 = a01 >> 16, v2 = a23 & 0xffffu, v3b = a23 >> 16;
    lds_w128(ke + 2 * (544 - p0), u32x4{v3b | (v2 << 16), v2 | (v2 << 16), v1 | (v0 << 16), v0 | (v0 << 16)});
  } else {
#pragma unroll
    for (int r = 0; r < 4; ++r) {
      const u32 pi = 2 * (c16 & 1) + (r & 1), nu = 32 + 16 * (g - 2) + 8 * (r >> 1) + (c16 >> 1);
      lds_w16(ke + 2 * (576 + 64 * pi + 80 - nu), pk_bf16(acc[1][r], 0.f));
    }
  }
  fence_c();
  {
    const u32 lb = 128 * (g >> 1) + 4 * (g & 1) + 8 * c16;
#pragma unroll
    for (int r = 0; r < 4; ++r) lds_w16(ke + 2 * (550 - lb - r), pk_bf16(acc[0][r], 0.f));
  }
  if (g < 2) {
#pragma unroll
    for (int r = 0; r < 4; ++r) {
      const u32 pi = c16 & 1, nu = 64 + 32 * g + 8 * r + (c16 >> 1);
      lds_w16(ke + 2 * (832 + 96 * pi + 144 - nu), pk_bf16(acc[2][r], 0.f));
    }
  } else if (lane == 32) {
    lds_w16(ke + 2 * (832 + 144 - 128), pk_bf16(acc[2][0], 0.f));
  }
  fence_c();
}

DEVI void conv_wave(const Params& p, u32 lds0, u32 cv, int lane, int d STAMP_ARG) {
  const u32 g = lane >> 4, r16 = lane & 15;
  const u32 ke = cv + CV_KE, ko = cv + CV_KO;
  const u32 dskip = p.Dskip[d];
  {
    u32 a[8], b[8];
#pragma unroll
    for (int j = 0; j < 8; ++j) { a[j] = lds_r32(ke + 4 * (64 * j + lane)); b[j] = lds_r32(ke + 4 * (64 * j + lane) + 4); }
#pragma unroll
    for (int j = 0; j < 8; ++j) lds_w32(ko + 4 * (64 * j + lane), __builtin_amdgcn_alignbit(b[j], a[j], 16));
  }
  fence_c();
  const u32 ub = cv + CV_U + 512;
  if (lane < 32) lds_w128(cv + CV_U + 16 * lane, u32x4{0, 0, 0, 0});
  else if (lane < 48) {
    const u32 arr = (lane >> 2) & 3;
    const u32 base = arr == 0 ? CV_W5 : arr == 1 ? CV_W3 : arr == 2 ? CV_W9 : CV_W7;
    lds_w128(cv + base + 16 * (lane & 3), u32x4{0, 0, 0, 0});
  }
  const u32 kpar = (r16 & 1) ? ko - 2 : ke;
  u32x4 A[17];
  {
    const u32 ab = kpar + 2 * (550 - 32 * 16 - 16 * (g >> 1) + 8 * (g & 1) - r16);
#pragma unroll
    for (int k = 0; k < 17; ++k) {
      const u32 a = ab + 64 * (16 - k);
      A[k] = u32x4{lds_r32(a), lds_r32(a + 4), lds_r32(a + 8), lds_r32(a + 12)};
    }
  }
  {
    const u64 tag = get_tag(lds0);
    poll_flags(p.ws, WS_FU + 8 * ((d / 80) * 32 + (lane & 31)), lane < 32, tag);
  }
  STAMP(7);
  const rsrc_t rut = mkrsrc(p.ws + WS_UT, D * L * 2);
  u32x4 uv[4];
#pragma unroll
  for (int j = 0; j < 4; ++j) uv[j] = bld128<SC1>(rut, d * 4096 + 1024 * j + 16 * lane, 0);
#pragma unroll
  for (int j = 0; j < 4; ++j) lds_w128(ub + 1024 * j + 16 * lane, uv[j]);
  fence_c();
  STAMP(8);
  {
    u32x2 pv[3], nx[3]; u32x4 cu[3];
#pragma unroll
    for (int it = 0; it < 3; ++it) {
      const u32 a = ub + 1024 * it + 16 * lane;
      pv[it] = lds_r64(a - 8); cu[it] = lds_r128(a); nx[it] = lds_r64(a + 16);
    }
    const u32x2 u0 = lds_r64(ub);
#pragma unroll
    for (int it = 0; it < 3; ++it) {
      float v[16];
      v[0] = bflo(pv[it].x); v[1] = bfhi(pv[it].x); v[2] = bflo(pv[it].y); v[3] = bfhi(pv[it].y);
#pragma unroll
      for (int e = 0; e < 4; ++e) { v[4 + 2 * e] = bflo(cu[it][e]); v[5 + 2 * e] = bfhi(cu[it][e]); }
      v[12] = bflo(nx[it].x); v[13] = bfhi(nx[it].x); v[14] = bflo(nx[it].y); v[15] = bfhi(nx[it].y);
      float w3[8], w5[8], w7[8], w9[8];
#pragma unroll
      for (int e = 0; e < 8; ++e) {
        w3[e] = v[4 + e] + (v[3 + e] + v[5 + e]);
        w5[e] = w3[e] + (v[2 + e] + v[6 + e]);
        if (it < 2) { w7[e] = w5[e] + (v[1 + e] + v[7 + e]); w9[e] = w7[e] + (v[e] + v[8 + e]); }
      }
      const u32 o = 64 + 1024 * it + 16 * lane;
      lds_w128(cv + CV_W5 + o, u32x4{pk_bf16(w5[0], w5[1]), pk_bf16(w5[2], w5[3]), pk_bf16(w5[4], w5[5]), pk_bf16(w5[6], w5[7])});
      lds_w128(cv + CV_W3 + o, u32x4{pk_bf16(w3[0], w3[1]), pk_bf16(w3[2], w3[3]), pk_bf16(w3[4], w3[5]), pk_bf16(w3[6], w3[7])});
      if (it < 2) {
        lds_w128(cv + CV_W9 + o, u32x4{pk_bf16(w9[0], w9[1]), pk_bf16(w9[2], w9[3]), pk_bf16(w9[4], w9[5]), pk_bf16(w9[6], w9[7])});
        lds_w128(cv + CV_W7 + o, u32x4{pk_bf16(w7[0], w7[1]), pk_bf16(w7[2], w7[3]), pk_bf16(w7[4], w7[5]), pk_bf16(w7[6], w7[7])});
      }
    }
    fence_c();
    const float a0 = bflo(u0.x), a1 = a0 + bfhi(u0.x), a2 = a1 + bflo(u0.y), a3 = a2 + bfhi(u0.y);
    if (lane == 0) {
      lds_w64(cv + CV_W9 + 64 - 8, u32x2{pk_bf16(a0, a1), pk_bf16(a2, a3)});
      lds_w64(cv + CV_W7 + 64 - 8, u32x2{pk_bf16(0.f, a0), pk_bf16(a1, a2)});
      lds_w64(cv + CV_W5 + 64 - 8, u32x2{0u, pk_bf16(a0, a1)});
      lds_w64(cv + CV_W3 + 64 - 8, u32x2{0u, pk_bf16(0.f, a0)});
    }
  }
  fence_c();
  STAMP(9);
  f32x4 acc[8];
#pragma unroll
  for (int t = 0; t < 8; ++t) acc[t] = f32x4{0, 0, 0, 0};
  {
    const u32 bb = ub + 2 * (16 * r16 - 16 * (g >> 1) + 8 * (g & 1));
    const u32 kc = (*(LAS u16*)(uintptr_t)(ke + 2 * (832 + 144 - 64))) ^ 0x8000u;
    const int idx0 = 1008 + 16 * (g >> 1) - 8 * (g & 1) + r16;
    u32x4 C;
#pragma unroll
    for (int e = 0; e < 4; ++e) {
      const int i0 = idx0 - 2 * e, i1 = i0 - 1;
      const u32 lo = (i0 == 1020 || i0 == 1021) ? kc : 0u, hi = (i1 == 1020 || i1 == 1021) ? kc : 0u;
      C[e] = lo | (hi << 16);
    }
    constexpr int PF = 6;
    u32x4 B[PF];
#pragma unroll
    for (int j = 0; j < PF; ++j) B[j] = lds_r128(bb + 64 * (j - 7));
    SB();
#pragma unroll
    for (int z = -7; z <= 56; ++z) {
      const int sl = (z + 7) % PF;
#pragma unroll
      for (int t = 0; t < 8; ++t) {
        const int k = 8 * t - z;
        const int kmax = t == 0 ? 7 : (t == 1 ? 15 : 16);
        if (k >= 0 && k <= kmax) acc[t] = mfma16(A[k], B[sl], acc[t]);
      }
      if (z + PF <= 56) B[sl] = lds_r128(bb + 64 * (z + PF));
      else if (z + PF - 57 < 5) B[sl] = lds_r128(bb + 2 * (256 * (z + PF - 57 + 3) - 16 * 63));
      SB();
    }
#pragma unroll
    for (int t = 3; t < 8; ++t) acc[t] = mfma16(C, B[(57 + t - 3 + 7) % PF], acc[t]);
  }
  STAMP(10);
  const u32 zero_a = cv + CV_U + 8 * (lane & 3);
  const u32 q = r16 >> 2, pp = lane & 3;
  {
    u32x4 T4[2][3], T8[5];
#pragma unroll
    for (int pr = 0; pr < 2; ++pr)
#pragma unroll
      for (int dl = 0; dl < 3; ++dl) {
        const u32 a = kpar + 2 * (576 + 64 * (2 * pr + (g >> 1)) + 80 - 16 * (dl + 2) - r16 + 4 * (g & 1));
        T4[pr][dl] = u32x4{lds_r32(a), lds_r32(a + 4), lds_r32(a + 16), lds_r32(a + 20)};
      }
#pragma unroll
    for (int dl = 0; dl < 5; ++dl) {
      const u32 a = kpar + 2 * (832 + 96 * (g >> 1) + 144 - 16 * (dl + 4) - r16 + 4 * (g & 1));
      T8[dl] = u32x4{lds_r32(a), lds_r32(a + 4), lds_r32(a + 16), lds_r32(a + 20)};
    }
    const u32 wb4 = cv + ((g >> 1) ? CV_W3 - 8 : CV_W5) + 64 + 2 * (16 * (4 * (g & 1) + q) + 4 * pp);
    const u32 wb8 = cv + ((g >> 1) ? CV_W7 - 16 : CV_W9) + 64 + 2 * (16 * (4 * (g & 1) + q) + 4 * pp);
    const bool edge = (g & 1) && q == 3;
    u32x4 A4[2][7], A8[5];
#pragma unroll
    for (int pr = 0; pr < 2; ++pr)
#pragma unroll
      for (int be = -1; be <= 5; ++be) {
        if (be == -1) {
          if (pr == 0) { const u32x2 hi = lds_tr(edge ? wb4 - 512 + 256 : zero_a); A4[0][0] = u32x4{0u, 0u, hi.x, hi.y}; }
        } else {
          const u32 a = wb4 + 512 * be - 16 * pr;
          A4[pr][be + 1] = cat(lds_tr(a), lds_tr(a + 256));
        }
      }
#pragma unroll
    for (int be = -1; be <= 3; ++be) {
      if (be == -1) { const u32x2 hi = lds_tr(edge ? wb8 - 512 + 256 : zero_a); A8[0] = u32x4{0u, 0u, hi.x, hi.y}; }
      else { const u32 a = wb8 + 512 * be; A8[be + 1] = cat(lds_tr(a), lds_tr(a + 256)); }
    }
#pragma unroll
    for (int be = -1; be <= 5; ++be)
#pragma unroll
      for (int pr = 0; pr < 2; ++pr) {
        if (be == -1 && pr == 1) continue;
#pragma unroll
        for (int dl = 0; dl < 3; ++dl) {
          const int t = be + dl + 2;
          if (t >= 1 && t <= 7) acc[t] = mfma16(A4[pr][be + 1], T4[pr][dl], acc[t]);
        }
      }
#pragma unroll
    for (int be = -1; be <= 3; ++be)
#pragma unroll
      for (int dl = 0; dl < 5; ++dl) {
        const int t = be + dl + 4;
        if (t >= 3 && t <= 7) acc[t] = mfma16(A8[be + 1], T8[dl], acc[t]);
      }
  }
  STAMP(11);
  fence_c();
  const float ds = bf2f(dskip);
  const u32 ob = cv + CV_W5;
  u32x2 uu[8];
#pragma unroll
  for (int t = 0; t < 8; ++t) uu[t] = lds_r64(ub + 2 * (256 * t + 16 * r16 + 4 * g));
#pragma unroll
  for (int t = 0; t < 8; ++t) {
    const u32 y01 = pk_bf16(acc[t][0], acc[t][1]), y23 = pk_bf16(acc[t][2], acc[t][3]);
    const u32 s01 = pk_bf16(bflo(uu[t].x) * ds, bfhi(uu[t].x) * ds), s23 = pk_bf16(bflo(uu[t].y) * ds, bfhi(uu[t].y) * ds);
    lds_w64(ob + 2 * (256 * t + 16 * r16 + 4 * g), u32x2{pk_bf16(bflo(y01) + bflo(s01), bfhi(y01) + bfhi(s01)), pk_bf16(bflo(y23) + bflo(s23), bfhi(y23) + bfhi(s23))});
  }
  fence_c();
  const rsrc_t rat = mkrsrc(p.ws + WS_AT, D * L * 2);
#pragma unroll
  for (int j = 0; j < 4; ++j) bst128<SC1>(rat, lds_r128(ob + 1024 * j + 16 * lane), d * 4096 + 1024 * j + 16 * lane, 0);
  const u64 tag = get_tag(lds0);
  wait_vm0();
  if (lane == 0) gst64(p.ws, WS_FA + 8 * d, tag);
}

DEVI void gemm2_main(const Params& p, int kw, int lane, int m2, int j, int rot, f32x4 (&acc)[4][5]) {
  const rsrc_t rh = mkrsrc(p.ws + WS_HF, 32 * 48 * 4 * 1024), rw = mkrsrc(p.ws + WS_W2F, 8 * 48 * 5 * 1024);
  const u32 vo = lane * 16;
  const u32 hb = (m2 * 48 + 12 * kw) * 4096, wb = (j * 48 + 12 * kw) * 5120;
#pragma unroll
  for (int m = 0; m < 4; ++m)
#pragma unroll
    for (int n = 0; n < 5; ++n) acc[m][n] = f32x4{0, 0, 0, 0};
  constexpr int DP = 3;
  u32x4 hf[DP][4], wf[DP][5];
  auto ks = [&](int s) { int t = s + rot; return t >= 12 ? t - 12 : t; };
#pragma unroll
  for (int s = 0; s < DP; ++s) {
    const int k = ks(s);
#pragma unroll
    for (int n = 0; n < 5; ++n) wf[s][n] = bld128<SC1>(rw, vo + 1024 * (n & 3), wb + k * 5120 + (n >> 2) * 4096);
#pragma unroll
    for (int m = 0; m < 4; ++m) hf[s][m] = bld128<SC1>(rh, vo + 1024 * m, hb + k * 4096);
  }
#pragma unroll
  for (int s = 0; s < 12; ++s) {
    const int sl = s % DP;
    const int k = ks(s + DP < 12 ? s + DP : 0);
#pragma unroll
    for (int n = 0; n < 5; ++n) {
#pragma unroll
      for (int m = 0; m < 4; ++m) {
        acc[m][n] = mfma16(wf[sl][n], hf[sl][m], acc[m][n]);
        if (n == 4 && s + DP < 12) { SB(); hf[sl][m] = bld128<SC1>(rh, vo + 1024 * m, hb + k * 4096); SB(); }
      }
      if (s + DP < 12) { SB(); wf[sl][n] = bld128<SC1>(rw, vo + 1024 * (n & 3), wb + k * 5120 + (n >> 2) * 4096); }
      SB();
    }
  }
}

__global__ __launch_bounds__(512, 2) void fused_kernel(Params p) {
  extern __shared__ __attribute__((aligned(16))) char smem[];
  const u32 lds0 = (u32)(uintptr_t)(LAS char*)smem;
  const int b = blockIdx.x, lane = threadIdx.x & 63, w = rfl(threadIdx.x >> 6);
  const int x = b & 7, i = b >> 3;
  const u32 sy = lds0 + LDS_SYNC;
  const int mt = 4 * (x >> 1) + (i >> 3), nt = 8 * (x & 1) + (i & 7);
  const int m2 = 4 * x + (i >> 3), j = i & 7;
  STAMP(0);
  if (threadIdx.x < 16) lds_w32(sy + 4 * threadIdx.x, 0);
  wait_lgkm0();
  __builtin_amdgcn_s_barrier();
  fence_c();
  f32x4 acc[4][5];
  if (w < 4) {
    __builtin_amdgcn_s_setprio(2);
    misc_job(p, lds0, w, lane, b);
    wait_lgkm0();
    sy_set(sy + 4 * SY_DONE + 4 * w, 1, lane);
    STAMP(1);
    gemm1_wave(p, lds0, w, lane, mt, nt, b STAMP_PASS);
    STAMP(5);
    sy_wait_ge(sy + 4 * SY_AUX, 1);
    sy_wait_ge(sy + 4 * SY_H, 1);
    STAMP(6);
    gemm2_main(p, w, lane, m2, j, (j + 3 * (i >> 3)) % 12, acc);
    STAMP(13);
#pragma unroll
    for (int m = 0; m < 4; ++m) {
      if (m == w) continue;
      const u32 slot = (w - m - 1) & 3;
#pragma unroll
      for (int n = 0; n < 5; ++n) lds_w128(lds0 + LDS_RED + ((m * 3 + slot) * 5 + n) * 1024 + 16 * lane, __builtin_bit_cast(u32x4, acc[m][n]));
    }
  } else if (w == 7) {
    __builtin_amdgcn_s_setprio(3);
    gemm1_loader(p, lds0, lane, mt, nt, ((i & 7) + 3 * (i >> 3)) % 10 STAMP_PASS);
    STAMP(5);
    const u64 tag = get_tag(lds0);
    {
      const int ca = (80 * j) / 128, cb = (80 * j + 79) / 128;
      poll_flags(p.ws, WS_FW + 8 * (ca * 64 + lane), lane < 48, tag);
      if (cb != ca) poll_flags(p.ws, WS_FW + 8 * (cb * 64 + lane), lane < 48, tag);
    }
    sy_set(sy + 4 * SY_AUX, 1, lane);
    STAMP(6);
    poll_flags(p.ws, WS_FH + 8 * (m2 * 32 + (lane & 31)), lane < 32, tag);
    sy_set(sy + 4 * SY_H, 1, lane);
    STAMP(7);
    poll_flags(p.ws, WS_FG + 8 * (m2 * 8 + (lane & 7)), lane < 8, tag);
    poll_flags(p.ws, WS_FA + 8 * (80 * j + lane), true, tag);
    poll_flags(p.ws, WS_FA + 8 * (80 * j + 64 + (lane & 15)), lane < 16, tag);
    STAMP(8);
    const rsrc_t rat = mkrsrc(p.ws + WS_AT, D * L * 2);
    u32x4 av[10];
#pragma unroll
    for (int jj = 0; jj < 10; ++jj) {
      const u32 id = 64 * jj + lane, row = id >> 3, c = id & 7;
      av[jj] = bld128<SC1>(rat, (80 * j + row) * 4096 + 128 * m2 + 16 * c, 0);
    }
    const float gv = __hip_atomic_load((GAS float*)(p.ws + WS_G) + 64 * m2 + lane, RLX_AGENT);
#pragma unroll
    for (int jj = 0; jj < 10; ++jj) {
      const u32 id = 64 * jj + lane, row = id >> 3, c = id & 7;
      lds_w128(lds0 + LDS_ATILE + row * 160 + 16 * c, av[jj]);
    }
    lds_w32(lds0 + LDS_GT + 4 * lane, __float_as_uint(gv));
    STAMP(13);
  } else {
    const int xw = w - 4, nch = b < 128 ? 3 : 2, d0 = b < 128 ? 3 * b : 384 + 2 * (b - 128);
    if (xw < nch) {
      const u32 cv = lds0 + LDS_CONV + xw * CV_SZ;
      kgen_wave(p, lds0, cv, lane, d0 + xw, xw == 0 STAMP_PASS);
      STAMP(5);
      conv_wave(p, lds0, cv, lane, d0 + xw STAMP_PASS);
      STAMP(13);
    }
  }
  wait_lgkm0();
  __builtin_amdgcn_s_barrier();
  fence_c();
  STAMP(14);
  if (w < 4) {
    const u32 g = lane >> 4, r16 = lane & 15;
    f32x4 own[5];
#pragma unroll
    for (int m = 0; m < 4; ++m)
      if (m == w) {
#pragma unroll
        for (int n = 0; n < 5; ++n) own[n] = acc[m][n];
      }
    const int t = 64 * m2 + 16 * w + r16;
    const float gv = __uint_as_float(lds_r32(lds0 + LDS_GT + 4 * (16 * w + r16)));
    const float og = rbf(1.f - gv);
    const rsrc_t ro = mkrsrc(p.out, L * D * 2);
#pragma unroll
    for (int n = 0; n < 5; ++n) {
#pragma unroll
      for (int s = 0; s < 3; ++s) own[n] += __builtin_bit_cast(f32x4, lds_r128(lds0 + LDS_RED + ((w * 3 + s) * 5 + n) * 1024 + 16 * lane));
      const u32x2 av = lds_tr(lds0 + LDS_ATILE + (16 * n + 4 * g + (r16 >> 2)) * 160 + 32 * w + 8 * (lane & 3));
      const u32 b01 = pk_bf16(own[n][0], own[n][1]), b23 = pk_bf16(own[n][2], own[n][3]);
      const u32 ga01 = pk_bf16(gv * bflo(av.x), gv * bfhi(av.x)), ga23 = pk_bf16(gv * bflo(av.y), gv * bfhi(av.y));
      const u32 gb01 = pk_bf16(og * bflo(b01), og * bfhi(b01)), gb23 = pk_bf16(og * bflo(b23), og * bfhi(b23));
      const u32x2 o = {pk_bf16(bflo(ga01) + bflo(gb01), bfhi(ga01) + bfhi(gb01)), pk_bf16(bflo(ga23) + bflo(gb23), bfhi(ga23) + bfhi(gb23))};
      bst64<0>(ro, o, t * 1280 + (80 * j + 16 * n + 4 * g) * 2, 0);
    }
    if (b == 0 && w == 0) { const u64 tag = get_tag(lds0); if (lane == 0) gst64(p.ws, WS_EP, tag); }
  }
}

extern "C" void kernel_launch(void* const* d_in, const int* in_sizes, int n_in, void* d_out, int out_size, void* d_ws, size_t ws_size,
                              hipStream_t stream) {
  Params p;
  p.u = (const u16*)d_in[0]; p.Are = (const u16*)d_in[1]; p.Aim = (const u16*)d_in[2]; p.Bre = (const u16*)d_in[3];
  p.Bim = (const u16*)d_in[4]; p.Cre = (const u16*)d_in[5]; p.Cim = (const u16*)d_in[6]; p.logdt = (const u16*)d_in[7];
  p.Dskip = (const u16*)d_in[8]; p.w1 = (const u16*)d_in[10]; p.w2 = (const u16*)d_in[11]; p.gw = (const u16*)d_in[12];
  p.out = (u16*)d_out; p.ws = (char*)d_ws;
  (void)hipFuncSetAttribute((const void*)fused_kernel, hipFuncAttributeMaxDynamicSharedMemorySize, LDS_TOTAL);
  fused_kernel<<<256, 512, LDS_TOTAL, stream>>>(p);
}
```

```cpp
#include <hip/hip_runtime.h>
#include <stdint.h>

typedef unsigned short u16;
typedef unsigned u32;
typedef unsigned long long u64;
typedef float f32x4 __attribute__((ext_vector_type(4)));
typedef float f32x2 __attribute__((ext_vector_type(2)));
typedef __bf16 bf16x8 __attribute__((ext_vector_type(8)));
typedef __bf16 bf16x2 __attribute__((ext_vector_type(2)));
typedef short s16x4 __attribute__((ext_vector_type(4)));
typedef unsigned u32x4 __attribute__((ext_vector_type(4)));
typedef unsigned u32x2 __attribute__((ext_vector_type(2)));
#define LAS __attribute__((address_space(3)))
#define GAS __attribute__((address_space(1)))
#define DEVI __device__ __forceinline__
#define SB() __builtin_amdgcn_sched_barrier(0)
#define RLX_AGENT __ATOMIC_RELAXED, __HIP_MEMORY_SCOPE_AGENT

constexpr int L = 2048, D = 640, NM = 192, F = 1536;
#define ROT1 0
#define G2SIG 8
#define PRIO_K 0
#define PRIO_G 2
#define FA_SLEEP 8
#define FU_SLEEP 16
constexpr int SC1 = 16;

constexpr u32 WS_HF   = 0;
constexpr u32 WS_W2F  = WS_HF + 32 * 48 * 4 * 1024;
constexpr u32 WS_UT   = WS_W2F + 8 * 48 * 5 * 1024;
constexpr u32 WS_AT   = WS_UT + D * L * 2;
constexpr u32 WS_KIMG = WS_AT + D * L * 2;
constexpr u32 WS_G    = WS_KIMG + D * 2048;
constexpr u32 WS_EP   = WS_G + L * 4;
constexpr u32 WS_FU   = WS_EP + 256;
constexpr u32 WS_FW   = WS_FU + 8 * 32 * 8;
constexpr u32 WS_FG   = WS_FW + 5 * 64 * 8;
constexpr u32 WS_FH   = WS_FG + 32 * 8 * 8;
constexpr u32 WS_FA   = WS_FH + 32 * 32 * 8;
constexpr u32 WS_END  = WS_FA + 640 * 8;

constexpr u32 STG = 28672;
constexpr u32 LDS_RED = 0;
constexpr u32 LDS_ATILE = 61440;
constexpr u32 LDS_GT = LDS_ATILE + 12800;
constexpr u32 LDS_CONV = 3 * STG;
constexpr u32 CV_U = 0;
constexpr u32 CV_W5 = 4608;
constexpr u32 CV_W3 = CV_W5 + 3136;
constexpr u32 CV_W9 = CV_W3 + 3136;
constexpr u32 CV_W7 = CV_W9 + 2112;
constexpr u32 CV_KE = CV_W7 + 2112;
constexpr u32 CV_KO = CV_KE + 2048 + 16;
constexpr u32 CV_SZ = CV_KO + 2048;
constexpr u32 CV_TAB = 0;
constexpr u32 LDS_SYNC = LDS_CONV + 3 * CV_SZ;
constexpr u32 SY_DONE = 0  , SY_FULL = 4, SY_AUX = 5, SY_H = 6, SY_MISC = 7, SY_TAG = 8  , SY_TAGV = 10, SY_SCR = 11, SY_G2 = 12;
constexpr u32 LDS_DBG = LDS_SYNC + 64;
constexpr u32 LDS_TOTAL = LDS_SYNC + 64 + 1024;
constexpr u32 SPIN_CAP = 1u << 20, POLL_CAP = 1u << 16;

struct Params {
  const u16 *u, *Are, *Aim, *Bre, *Bim, *Cre, *Cim, *logdt, *Dskip, *w1, *w2, *gw;
  u16* out;
  char* ws;
};

#define STAMP(k) do {} while (0)
#define STAMP_ARG
#define TRACE(i) do {} while (0)
#define STAMP_PASS

DEVI float bf2f(u32 x16) { return __uint_as_float(x16 << 16); }
DEVI float bflo(u32 x) { return __uint_as_float(x << 16); }
DEVI float bfhi(u32 x) { return __uint_as_float(x & 0xffff0000u); }
DEVI u32 pk_bf16(float lo, float hi) {
  bf16x2 v = {(__bf16)lo, (__bf16)hi};
  return __builtin_bit_cast(u32, v);
}
DEVI float rbf(float x) { return bflo(pk_bf16(x, x) & 0xffffu); }
DEVI float dot2bf(u32 a, u32 b, float c) { return __builtin_amdgcn_fdot2_f32_bf16(__builtin_bit_cast(bf16x2, a), __builtin_bit_cast(bf16x2, b), c, false); }
DEVI f32x4 mfma16(u32x4 a, u32x4 b, f32x4 c) {
  return __builtin_amdgcn_mfma_f32_16x16x32_bf16(__builtin_bit_cast(bf16x8, a), __builtin_bit_cast(bf16x8, b), c, 0, 0, 0);
}
DEVI void mfma16t(f32x4& acc, u32x4 a, u32x4 b) { asm volatile("v_mfma_f32_16x16x32_bf16 %0, %1, %2, %0" : "+v"(acc) : "v"(a), "v"(b)); }
DEVI void mfma16z(f32x4& acc, u32x4 a, u32x4 b) { asm volatile("v_mfma_f32_16x16x32_bf16 %0, %1, %2, 0" : "=&v"(acc) : "v"(a), "v"(b)); }
DEVI u32x4 lds_r128(u32 a) { return *(LAS u32x4*)(uintptr_t)a; }
DEVI u32x2 lds_r64(u32 a) { return *(LAS u32x2*)(uintptr_t)a; }
DEVI u32 lds_r32(u32 a) { return *(LAS u32*)(uintptr_t)a; }
DEVI void lds_w128(u32 a, u32x4 v) { *(LAS u32x4*)(uintptr_t)a = v; }
DEVI void lds_w64(u32 a, u32x2 v) { *(LAS u32x2*)(uintptr_t)a = v; }
DEVI void lds_w32(u32 a, u32 v) { *(LAS u32*)(uintptr_t)a = v; }
DEVI void lds_w16(u32 a, u32 v) { *(LAS u16*)(uintptr_t)a = (u16)v; }
DEVI u32 lds_vr32(u32 a) { return *(volatile LAS u32*)(uintptr_t)a; }
DEVI u32x4 lds_vr128(u32 a) { return *(volatile LAS u32x4*)(uintptr_t)a; }
DEVI void lds_vw32(u32 a, u32 v) { *(volatile LAS u32*)(uintptr_t)a = v; }
DEVI u32x2 lds_tr(u32 a) { return __builtin_bit_cast(u32x2, __builtin_amdgcn_ds_read_tr16_b64_v4i16((LAS s16x4*)(uintptr_t)a)); }
DEVI u32x4 cat(u32x2 a, u32x2 b) { return u32x4{a.x, a.y, b.x, b.y}; }
typedef __amdgpu_buffer_rsrc_t rsrc_t;
DEVI rsrc_t mkrsrc(const void* p, u32 bytes) { return __builtin_amdgcn_make_buffer_rsrc((void*)p, 0, bytes, 0x00020000); }
template <int AUX> DEVI u32x4 bld128(rsrc_t r, u32 v, u32 s) { return __builtin_amdgcn_raw_buffer_load_b128(r, v, s, AUX); }
template <int AUX> DEVI void bst128(rsrc_t r, u32x4 d, u32 v, u32 s) { __builtin_amdgcn_raw_buffer_store_b128(d, r, v + s, 0, AUX); }
template <int AUX> DEVI void bst64(rsrc_t r, u32x2 d, u32 v, u32 s) { __builtin_amdgcn_raw_buffer_store_b64(d, r, v + s, 0, AUX); }
DEVI u32 rfl(u32 x) { return __builtin_amdgcn_readfirstlane(x); }
DEVI void wait_lgkm0() { asm volatile("s_waitcnt lgkmcnt(0)" ::: "memory"); }
DEVI void wait_vm0() { asm volatile("s_waitcnt vmcnt(0)" ::: "memory"); }
DEVI void fence_c() { asm volatile("" ::: "memory"); }
DEVI u32 min4(u32x4 f) { return min(min(f.x, f.y), min(f.z, f.w)); }
DEVI void sy_set(u32 a, u32 v, int lane) { if (lane == 0) lds_vw32(a, v); }
DEVI void sy_wait_ge(u32 a, u32 v) { u32 n = 0; while (rfl(lds_vr32(a)) < v && ++n < SPIN_CAP) {} fence_c(); }
DEVI u64 gld64(const char* ws, u32 off) { return __hip_atomic_load((GAS u64*)(ws + off), RLX_AGENT); }
DEVI void gst64(char* ws, u32 off, u64 v) { __hip_atomic_store((GAS u64*)(ws + off), v, RLX_AGENT); }
DEVI u64 get_tag(u32 lds0) {
  sy_wait_ge(lds0 + LDS_SYNC + 4 * SY_TAGV, 1);
  const u32x2 t = *(volatile LAS u32x2*)(uintptr_t)(lds0 + LDS_SYNC + 4 * SY_TAG);
  return ((u64)rfl(t.y) << 32) | rfl(t.x);
}
template <int SLEEP = 4>
DEVI void poll_flags(const char* ws, u32 off, bool act, u64 tag) {
  for (u32 n = 0; n < POLL_CAP; ++n) {
    const u64 v = act ? gld64(ws, off) : tag;
    if (__all(v == tag)) break;
    __builtin_amdgcn_s_sleep(SLEEP);
  }
  fence_c();
}

DEVI void cmul2(f32x2& r0, f32x2 a0, f32x2 b0, f32x2& r1, f32x2 a1, f32x2 b1) {
  f32x2 t0, t1;
  asm("v_pk_mul_f32 %2, %4, %5 op_sel:[1,1] op_sel_hi:[1,0] neg_lo:[0,1]\n\t"
      "v_pk_mul_f32 %3, %6, %7 op_sel:[1,1] op_sel_hi:[1,0] neg_lo:[0,1]\n\t"
      "v_pk_fma_f32 %0, %4, %5, %2 op_sel:[0,0,0] op_sel_hi:[0,1,1]\n\t"
      "v_pk_fma_f32 %1, %6, %7, %3 op_sel:[0,0,0] op_sel_hi:[0,1,1]"
      : "=&v"(r0), "=&v"(r1), "=&v"(t0), "=&v"(t1) : "v"(a0), "v"(b0), "v"(a1), "v"(b1));
}
DEVI f32x2 cexp_pow(float ex, float rev, float m) {
  float e = __builtin_amdgcn_exp2f(ex * m);
  float fr = __builtin_amdgcn_fractf(rev * m);
  return f32x2{e * __builtin_amdgcn_cosf(fr), e * __builtin_amdgcn_sinf(fr)};
}
DEVI f32x2 conj(f32x2 a) { return f32x2{a.x, -a.y}; }
DEVI u32 pk2(f32x2 v) { return pk_bf16(v.x, v.y); }

struct T_ { static constexpr bool value = true; };
struct F_ { static constexpr bool value = false; };

DEVI void misc_load(const Params& p, int w, int lane, int b, u32x4 (&ra)[10], u32x4 (&rb)[10]) {
#pragma unroll
  for (int j = 0; j < 10; ++j) { ra[j] = u32x4{0, 0, 0, 0}; rb[j] = u32x4{0, 0, 0, 0}; }
  const int x = b & 7, i = b >> 3, rq = x >> 1, fh = x & 1;
  const u32 g = lane >> 4;
  const rsrc_t rx = mkrsrc(p.u, L * D * 2);
  if (w < 2) {
    const int tt = 8 * rq + (i >> 2), dtile = 4 * fh + (i & 3);
#pragma unroll
    for (int j = 0; j < 5; ++j) {
      const u32 id = 64 * j + lane, row = id / 10, c = id % 10;
      ra[j] = bld128<0>(rx, (64 * tt + 32 * w + row) * 1280 + 160 * dtile + 16 * c, 0);
    }
  } else if (w == 2) {
    const rsrc_t rw2 = mkrsrc(p.w2, F * D * 2);
    const int kap = b % 48, c5 = b / 48;
#pragma unroll
    for (int j = 0; j < 8; ++j) ra[j] = bld128<0>(rw2, (32 * kap + 4 * j + g) * 1280 + 256 * c5 + 16 * (lane & 15), 0);
  } else {
    const int grow = 512 * rq + 256 * fh + 8 * i + (lane >> 3);
    const rsrc_t rg = mkrsrc(p.gw, D * 2);
#pragma unroll
    for (int j = 0; j < 10; ++j) {
      ra[j] = bld128<0>(rx, grow * 1280 + 16 * ((lane & 7) + 8 * j), 0);
      rb[j] = bld128<0>(rg, 16 * ((lane & 7) + 8 * j), 0);
    }
  }
}
DEVI void misc_process(const Params& p, u32 lds0, int w, int lane, int b, const u32x4 (&ra)[10], const u32x4 (&rb)[10]) {
  const int x = b & 7, i = b >> 3, rq = x >> 1, fh = x & 1;
  const u32 g = lane >> 4, q = (lane & 15) >> 2, pp = lane & 3;
  if (w < 2) {
    const u32 ms = lds0 + LDS_CONV + w * CV_SZ + 9216;
    const int tt = 8 * rq + (i >> 2), dtile = 4 * fh + (i & 3);
#pragma unroll
    for (int j = 0; j < 5; ++j) {
      const u32 id = 64 * j + lane, row = id / 10, c = id % 10;
      lds_w128(ms + row * 176 + 16 * c, ra[j]);
    }
    fence_c();
    const rsrc_t rut = mkrsrc(p.ws + WS_UT, D * L * 2);
#pragma unroll
    for (int dtl = 0; dtl < 5; ++dtl) {
      const u32 a = ms + (8 * g + q) * 176 + 32 * dtl + 8 * pp;
      const u32x4 v = cat(lds_tr(a), lds_tr(a + 4 * 176));
      bst128<SC1>(rut, v, (80 * dtile + 16 * dtl + (lane & 15)) * 4096 + (64 * tt + 32 * w + 8 * g) * 2, 0);
    }
  } else if (w == 2) {
    if (b < 240) {
      const u32 ms = lds0 + LDS_CONV + 2 * CV_SZ + 9216;
      const rsrc_t rwf = mkrsrc(p.ws + WS_W2F, 8 * 48 * 5 * 1024);
      const int kap = b % 48, c5 = b / 48;
#pragma unroll
      for (int hf = 0; hf < 2; ++hf) {
        fence_c();
        if (((lane >> 3) & 1) == hf) {
#pragma unroll
          for (int j = 0; j < 8; ++j) lds_w128(ms + (4 * j + g) * 160 + 16 * (lane & 7), ra[j]);
        }
        fence_c();
#pragma unroll
        for (int T = 0; T < 4; ++T) {
          const u32 a = ms + (4 * g + q) * 160 + 32 * T + 8 * pp;
          const u32x4 v = cat(lds_tr(a), lds_tr(a + 16 * 160));
          const int Tg = 8 * c5 + 4 * hf + T, j = Tg / 5, nu = Tg % 5;
          bst128<SC1>(rwf, v, lane * 16, ((j * 48 + kap) * 5 + nu) * 1024);
        }
      }
    }
  } else {
    const int grow = 512 * rq + 256 * fh + 8 * i + (lane >> 3);
    float s0 = 0.f, s1 = 0.f;
#pragma unroll
    for (int j = 0; j < 10; ++j)
#pragma unroll
      for (int e = 0; e < 4; e += 2) {
        s0 = dot2bf(ra[j][e], rb[j][e], s0);
        s1 = dot2bf(ra[j][e + 1], rb[j][e + 1], s1);
      }
    float s = s0 + s1;
    s += __shfl_xor(s, 1); s += __shfl_xor(s, 2); s += __shfl_xor(s, 4);
    if ((lane & 7) == 0) {
      const float sb = rbf(s);
      const float gv = rbf(1.f / (1.f + __expf(-sb)));
      __hip_atomic_store((GAS float*)(p.ws + WS_G) + grow, gv, RLX_AGENT);
    }
  }
}

DEVI void gemm1_loader(const Params& p, u32 lds0, int lane, int mt, int nt, int rot STAMP_ARG) {
  const rsrc_t rx = mkrsrc(p.u, L * D * 2), rw = mkrsrc(p.w1, D * F * 2);
  const u32 vx = (u32)(128 * mt + (lane >> 3)) * 1280 + (((lane & 7) ^ (lane >> 3)) & 7) * 16;
  u32 vw[3];
#pragma unroll
  for (int q = 0; q < 3; ++q) {
    const u32 o = 1024 * q + 16 * lane, row = o / 192, pc = (o % 192) >> 4;
    vw[q] = row * 3072 + 192 * nt + 16 * (pc ^ (2 * ((row >> 3) & 1)));
  }
  const u32 sy = lds0 + LDS_SYNC;
  u32 st = 0;
  int cc = rot;
  for (int k = 0; k < 10; ++k) {
    TRACE(4 * k);
    if (k >= 3) {
      u32 n = 0;
      while (rfl(min4(lds_vr128(sy + 4 * SY_DONE))) < (u32)(k - 2) && ++n < SPIN_CAP) {}
      fence_c();
    }
    TRACE(4 * k + 1);
    u32 m = lds0 + st, sx = cc * 128, sw = cc * 196608;
    asm volatile(
        "s_mov_b32 m0, %[m]\n\t"
        ".rept 14\n\t"
        "s_nop 0\n\t"
        "buffer_load_dwordx4 %[vx], %[rx], %[sx] offen lds\n\t"
        "s_add_u32 m0, m0, 0x400\n\t"
        "s_add_u32 %[sx], %[sx], 10240\n\t"
        ".endr"
        : [sx] "+s"(sx) : [m] "s"(m), [vx] "v"(vx), [rx] "s"(rx) : "memory", "scc");
    if (k >= 1) {
      asm volatile("s_waitcnt vmcnt(14)" ::: "memory");
      sy_set(sy + 4 * SY_FULL, k, lane);
    }
    m += 14 * 1024;
    asm volatile(
        "s_mov_b32 m0, %[m]\n\t"
        ".rept 2\n\t"
        "s_nop 0\n\t"
        "buffer_load_dwordx4 %[vx], %[rx], %[sx] offen lds\n\t"
        "s_add_u32 m0, m0, 0x400\n\t"
        "s_add_u32 %[sx], %[sx], 10240\n\t"
        ".endr\n\t"
        ".rept 4\n\t"
        "buffer_load_dwordx4 %[v0], %[rw], %[sw] offen lds\n\t"
        "s_add_u32 m0, m0, 0x400\n\t"
        "s_nop 0\n\t"
        "buffer_load_dwordx4 %[v1], %[rw], %[sw] offen lds\n\t"
        "s_add_u32 m0, m0, 0x400\n\t"
        "s_nop 0\n\t"
        "buffer_load_dwordx4 %[v2], %[rw], %[sw] offen lds\n\t"
        "s_add_u32 m0, m0, 0x400\n\t"
        "s_add_u32 %[sw], %[sw], 49152\n\t"
        ".endr"
        : [sx] "+s"(sx), [sw] "+s"(sw)
        : [m] "s"(m), [vx] "v"(vx), [v0] "v"(vw[0]), [v1] "v"(vw[1]), [v2] "v"(vw[2]), [rx] "s"(rx), [rw] "s"(rw)
        : "memory", "scc");
    st = st == 2 * STG ? 0 : st + STG;
    cc = cc == 9 ? 0 : cc + 1;
  }
  wait_vm0();
  sy_set(sy + 4 * SY_FULL, 10, lane);
}

DEVI void gemm1_wave(const Params& p, u32 lds0, int w, int lane, int mt, int nt, int b STAMP_ARG) {
  u32x4 mra[10], mrb[10];
  __builtin_amdgcn_s_sleep(6);
  misc_load(p, w, lane, b, mra, mrb);
  const u32 g = lane >> 4, r16 = lane & 15;
  const u32 sy = lds0 + LDS_SYNC;
  const u32 xb0 = lds0 + (32 * w + r16) * 128 + 16 * (g ^ (lane & 7));
  const u32 xb1 = lds0 + (32 * w + r16) * 128 + 16 * ((4 + g) ^ (lane & 7));
  const u32 wrow = lds0 + 16384 + (8 * g + (r16 >> 2)) * 192 + 16 * ((lane & 3) >> 1) + 8 * (lane & 1);
  const u32 wbe = wrow + 32 * (g & 1), wbo = wrow - 32 * (g & 1);
  f32x4 acc[6][2];
#pragma unroll
  for (int a = 0; a < 6; ++a) { acc[a][0] = f32x4{0, 0, 0, 0}; acc[a][1] = f32x4{0, 0, 0, 0}; }
  u32x4 xa[2], wa[6], xb[2], wb[6];
  auto rd = [&](int j, u32 st, int ks, u32x4 (&X)[2], u32x4 (&W)[6]) {
    if (j < 2) X[j] = lds_r128((ks ? xb1 : xb0) + st + 2048 * j);
    else {
      const int a = (j - 2) >> 1, h = (j - 2) & 1;
      const u32x2 v = lds_tr(((a & 1) ? wbo : wbe) + st + 32 * a + ks * 32 * 192 + h * 4 * 192);
      if (h == 0) { W[a].x = v.x; W[a].y = v.y; } else { W[a].z = v.x; W[a].w = v.y; }
    }
  };
  auto chunk = [&](int c, u32 st, u32 stn, auto LAST) {
    constexpr bool last = decltype(LAST)::value;
#pragma unroll
    for (int m = 0; m < 12; ++m) {
      acc[m >> 1][m & 1] = mfma16(wa[m >> 1], xa[m & 1], acc[m >> 1][m & 1]);
      rd(m, st, 1, xb, wb);
      if (m >= 10) rd(m + 2, st, 1, xb, wb);
      SB();
    }
    wait_lgkm0();
    u32 f = 0;
    if (!last) { sy_set(sy + 4 * SY_DONE + 4 * w, c + 1, lane); f = lds_vr32(sy + 4 * SY_FULL); }
    SB();
#pragma unroll
    for (int m = 0; m < 4; ++m) acc[m >> 1][m & 1] = mfma16(wb[m >> 1], xb[m & 1], acc[m >> 1][m & 1]);
    SB();
    if (!last) {
      u32 n = 0;
      if (w == 0) TRACE(64 + 2 * c);
      while (rfl(f) < (u32)(c + 2) && ++n < SPIN_CAP) f = lds_vr32(sy + 4 * SY_FULL);
      fence_c();
      if (w == 0) TRACE(64 + 2 * c + 1);
    }
#pragma unroll
    for (int m = 4; m < 12; ++m) {
      if (!last && m < 11) { rd(2 * (m - 4), stn, 0, xa, wa); rd(2 * (m - 4) + 1, stn, 0, xa, wa); }
      acc[m >> 1][m & 1] = mfma16(wb[m >> 1], xb[m & 1], acc[m >> 1][m & 1]);
      SB();
    }
  };
  sy_wait_ge(sy + 4 * SY_FULL, 1);
  STAMP(2);
#pragma unroll
  for (int j = 0; j < 14; ++j) rd(j, 0, 0, xa, wa);
  u32 st = 0;
  for (int c = 0; c < 9; ++c) {
    const u32 stn = st == 2 * STG ? 0 : st + STG;
    chunk(c, st, stn, F_{});
    st = stn;
    if (c == 0) {
      STAMP(1);
      misc_process(p, lds0, w, lane, b, mra, mrb);
      wait_lgkm0();
      if (lane == 0) __hip_atomic_fetch_add((LAS u32*)(uintptr_t)(sy + 4 * SY_SCR), 1u, __ATOMIC_RELAXED, __HIP_MEMORY_SCOPE_WORKGROUP);
    }
    if (c == 3) {
      wait_vm0();
      u32 old = 0;
      if (lane == 0) old = __hip_atomic_fetch_add((LAS u32*)(uintptr_t)(sy + 4 * SY_MISC), 1u, __ATOMIC_RELAXED, __HIP_MEMORY_SCOPE_WORKGROUP);
      if (rfl(old) == 3) {
        const u64 tag = get_tag(lds0);
        const int x = b & 7, i = b >> 3;
        const int tt = 8 * (x >> 1) + (i >> 2), dtile = 4 * (x & 1) + (i & 3);
        const int grow8 = 64 * (x >> 1) + 32 * (x & 1) + i;
        const u32 off = lane == 0 ? WS_FU + 8 * (dtile * 32 + tt) : lane == 1 ? WS_FG + 8 * grow8 : WS_FW + 8 * ((b / 48) * 64 + b % 48);
        if (lane < (b < 240 ? 3 : 2)) gst64(p.ws, off, tag);
      }
    }
    if (c == 4) STAMP(3);
  }
  chunk(9, st, 0, T_{});
  STAMP(4);
  const rsrc_t rh = mkrsrc(p.ws + WS_HF, 32 * 48 * 4 * 1024);
  const int m2 = 2 * mt + (w >> 1);
#pragma unroll
  for (int t = 0; t < 2; ++t)
#pragma unroll
    for (int k = 0; k < 3; ++k) {
      const f32x4 a = acc[2 * k][t], c = acc[2 * k + 1][t];
      const u32x4 v = {pk_bf16(fmaxf(a[0], 0.f), fmaxf(a[1], 0.f)), pk_bf16(fmaxf(a[2], 0.f), fmaxf(a[3], 0.f)),
                       pk_bf16(fmaxf(c[0], 0.f), fmaxf(c[1], 0.f)), pk_bf16(fmaxf(c[2], 0.f), fmaxf(c[3], 0.f))};
      bst128<SC1>(rh, v, lane * 16, ((m2 * 48 + 3 * nt + k) * 4 + (w & 1) * 2 + t) * 1024);
    }
}

DEVI void kgen_wave(const Params& p, u32 lds0, u32 cv, int lane, int d, bool first STAMP_ARG) {
  const u32 g = lane >> 4, c16 = lane & 15;
  u32 pr[3][6];
  {
    const u16* src[6] = {p.Are, p.Aim, p.Bre, p.Bim, p.Cre, p.Cim};
#pragma unroll
    for (int s = 0; s < 3; ++s)
#pragma unroll
      for (int a = 0; a < 6; ++a) pr[s][a] = src[a][d * NM + 64 * s + lane];
  }
  const float dt = rbf(__builtin_amdgcn_exp2f(bf2f(p.logdt[d]) * 1.44269504088896341f));
  if (first) {
    const u64 tag = *(const GAS u64*)(p.ws + WS_EP) + 1;
    if (lane == 0) { lds_vw32(lds0 + LDS_SYNC + 4 * SY_TAG, (u32)tag); lds_vw32(lds0 + LDS_SYNC + 4 * SY_TAG + 4, (u32)(tag >> 32)); lds_vw32(lds0 + LDS_SYNC + 4 * SY_TAGV, 1); }
  }
  const u32 tw = cv + CV_TAB + 4 * lane;
  const u32 tr = cv + CV_TAB + c16 * 288 + 16 * g;
  f32x4 acc[3] = {f32x4{0, 0, 0, 0}, f32x4{0, 0, 0, 0}, f32x4{0, 0, 0, 0}};
  lds_w32(tw, 0x00003f80u);
  STAMP(1);
#pragma unroll 1
  for (int s = 0; s < 3; ++s) {
    const u32 a0 = s == 0 ? pr[0][0] : s == 1 ? pr[1][0] : pr[2][0], a1 = s == 0 ? pr[0][1] : s == 1 ? pr[1][1] : pr[2][1];
    const u32 a2 = s == 0 ? pr[0][2] : s == 1 ? pr[1][2] : pr[2][2], a3 = s == 0 ? pr[0][3] : s == 1 ? pr[1][3] : pr[2][3];
    const u32 a4 = s == 0 ? pr[0][4] : s == 1 ? pr[1][4] : pr[2][4], a5 = s == 0 ? pr[0][5] : s == 1 ? pr[1][5] : pr[2][5];
    const float xr = bf2f(a0) * dt, xi = bf2f(a1) * dt, br = bf2f(a2) * dt, bi = bf2f(a3) * dt, cr = bf2f(a4), ci = bf2f(a5);
    const float ex = xr * 1.44269504088896341f, rev = xi * 0.15915494309189535f;
    const f32x2 w1 = cexp_pow(ex, rev, 1.f), w8 = cexp_pow(ex, rev, 8.f), w128 = cexp_pow(ex, rev, 128.f);
    const f32x2 gb = {cr * br - ci * bi, -(cr * bi + ci * br)};
    const f32x2 c1 = conj(w1), c128 = conj(w128);
    f32x2 c2, c4, w16, c256, c512, c1024;
    cmul2(c2, c1, c1, c256, c128, c128);
    cmul2(w16, w8, w8, c512, c256, c256);
    cmul2(c4, c2, c2, c1024, c512, c512);
    {
      f32x2 o = w8, e = w16, v = gb, v2;
      lds_w32(tw + 288 * 1, pk2(o));
      lds_w32(tw + 288 * 2, pk2(e));
      lds_w32(tw + 288 * 16, pk2(v));
#pragma unroll
      for (int r = 0; r < 7; ++r) {
        f32x2 on, vn;
        cmul2(on, o, w16, v2, v, c128);
        lds_w32(tw + 288 * (3 + 2 * r), pk2(on));
        lds_w32(tw + 288 * (24 + r), pk2(v2));
        if (r < 6) {
          f32x2 en;
          cmul2(en, e, w16, vn, v, c1);
          lds_w32(tw + 288 * (4 + 2 * r), pk2(en));
          e = en;
        } else {
          f32x2 dummy;
          cmul2(dummy, v, c1, vn, v, c1);
        }
        lds_w32(tw + 288 * (17 + r), pk2(vn));
        o = on; v = vn;
      }
      f32x2 a, bq;
      cmul2(v2, v, c128, a, gb, c256);
      lds_w32(tw + 288 * 31, pk2(v2));
      fence_c();
      u32x4 fb[4];
#pragma unroll
      for (int k = 0; k < 4; ++k) fb[k] = lds_r128(tr + 64 * k);
      u32x4 fa[4];
#pragma unroll
      for (int k = 0; k < 4; ++k) fa[k] = lds_r128(tr + 16 * 288 + 64 * k);
      fence_c();
      cmul2(bq, gb, c512, v, gb, c1024);
      f32x2 t1[16];
      t1[0] = a; t1[8] = bq;
      cmul2(t1[1], a, c2, t1[9], bq, c4);
      cmul2(t1[2], t1[1], c2, t1[4], a, c128);
      cmul2(t1[3], t1[2], c2, t1[5], t1[1], c128);
      cmul2(t1[6], t1[2], c128, t1[7], t1[3], c128);
      cmul2(t1[10], t1[8], c128, t1[11], t1[9], c128);
      cmul2(t1[12], t1[10], c128, t1[13], t1[11], c128);
      cmul2(t1[14], t1[12], c128, t1[15], t1[13], c128);
#pragma unroll
      for (int k = 0; k < 4; ++k) acc[0] = mfma16(fa[k], fb[k], acc[0]);
#pragma unroll
      for (int r = 0; r < 16; ++r) lds_w32(tw + 288 * (16 + r), pk2(t1[r]));
      fence_c();
#pragma unroll
      for (int k = 0; k < 4; ++k) fa[k] = lds_r128(tr + 16 * 288 + 64 * k);
      fence_c();
      f32x2 t2[9];
      t2[0] = v;
      f32x2 dm;
      cmul2(t2[1], v, c128, t2[2], v, c256);
      cmul2(t2[3], t2[1], c256, t2[4], t2[2], c256);
      cmul2(t2[5], t2[3], c256, t2[6], t2[4], c256);
      cmul2(t2[7], t2[5], c256, t2[8], t2[6], c256);
#pragma unroll
      for (int k = 0; k < 4; ++k) acc[1] = mfma16(fa[k], fb[k], acc[1]);
#pragma unroll
      for (int r = 0; r < 9; ++r) lds_w32(tw + 288 * (16 + r), pk2(t2[r]));
      fence_c();
#pragma unroll
      for (int k = 0; k < 4; ++k) fa[k] = lds_r128(tr + 16 * 288 + 64 * k);
      fence_c();
#pragma unroll
      for (int k = 0; k < 4; ++k) acc[2] = mfma16(fa[k], fb[k], acc[2]);
      (void)dm;
    }
  }
  STAMP(4);
  const u32 ke = cv + CV_KE;
  lds_w128(ke + 16 * lane, u32x4{0, 0, 0, 0});
  lds_w128(ke + 1024 + 16 * lane, u32x4{0, 0, 0, 0});
  fence_c();
  const float k512 = rbf(__int_as_float(__builtin_amdgcn_readlane(__float_as_int(acc[1][0]), 32)));
  if (g < 2) {
    const u32 p0 = 256 + 128 * g + 8 * c16;
    float v3 = acc[1][3];
    if (lane == 31) v3 -= k512;
    const u32 a01 = pk_bf16(acc[1][0], acc[1][1]), a23 = pk_bf16(acc[1][2], v3);
    const u32 v0 = a01 & 0xffffu, v1 = a01 >> 16, v2 = a23 & 0xffffu, v3b = a23 >> 16;
    lds_w128(ke + 2 * (544 - p0), u32x4{v3b | (v2 << 16), v2 | (v2 << 16), v1 | (v0 << 16), v0 | (v0 << 16)});
  } else {
#pragma unroll
    for (int r = 0; r < 4; ++r) {
      const u32 pi = 2 * (c16 & 1) + (r & 1), nu = 32 + 16 * (g - 2) + 8 * (r >> 1) + (c16 >> 1);
      lds_w16(ke + 2 * (576 + 64 * pi + 80 - nu), pk_bf16(acc[1][r], 0.f));
    }
  }
  fence_c();
  {
    const u32 lb = 128 * (g >> 1) + 4 * (g & 1) + 8 * c16;
#pragma unroll
    for (int r = 0; r < 4; ++r) lds_w16(ke + 2 * (550 - lb - r), pk_bf16(acc[0][r], 0.f));
  }
  if (g < 2) {
#pragma unroll
    for (int r = 0; r < 4; ++r) {
      const u32 pi = c16 & 1, nu = 64 + 32 * g + 8 * r + (c16 >> 1);
      lds_w16(ke + 2 * (832 + 96 * pi + 144 - nu), pk_bf16(acc[2][r], 0.f));
    }
  } else if (lane == 32) {
    lds_w16(ke + 2 * (832 + 144 - 128), pk_bf16(acc[2][0], 0.f));
  }
  fence_c();
}

DEVI void conv_wave(const Params& p, u32 lds0, u32 cv, int lane, int d STAMP_ARG) {
  const u32 g = lane >> 4, r16 = lane & 15;
  const u32 ke = cv + CV_KE, ko = cv + CV_KO;
  const u32 dskip = p.Dskip[d];
  {
    u32 a[8], b[8];
#pragma unroll
    for (int j = 0; j < 8; ++j) { a[j] = lds_r32(ke + 4 * (64 * j + lane)); b[j] = lds_r32(ke + 4 * (64 * j + lane) + 4); }
#pragma unroll
    for (int j = 0; j < 8; ++j) lds_w32(ko + 4 * (64 * j + lane), __builtin_amdgcn_alignbit(b[j], a[j], 16));
  }
  sy_wait_ge(lds0 + LDS_SYNC + 4 * SY_SCR, 4);
  const u32 ub = cv + CV_U + 512;
  if (lane < 32) lds_w128(cv + CV_U + 16 * lane, u32x4{0, 0, 0, 0});
  else if (lane < 48) {
    const u32 arr = (lane >> 2) & 3;
    const u32 base = arr == 0 ? CV_W5 : arr == 1 ? CV_W3 : arr == 2 ? CV_W9 : CV_W7;
    lds_w128(cv + base + 16 * (lane & 3), u32x4{0, 0, 0, 0});
  }
  const u32 kpar = (r16 & 1) ? ko - 2 : ke;
  u32x4 A[17];
  {
    const u32 ab = kpar + 2 * (550 - 32 * 16 - 16 * (g >> 1) + 8 * (g & 1) - r16);
#pragma unroll
    for (int k = 0; k < 17; ++k) {
      const u32 a = ab + 64 * (16 - k);
      A[k] = u32x4{lds_r32(a), lds_r32(a + 4), lds_r32(a + 8), lds_r32(a + 12)};
    }
  }
  {
    const u64 tag = get_tag(lds0);
    poll_flags<FU_SLEEP>(p.ws, WS_FU + 8 * ((d / 80) * 32 + (lane & 31)), lane < 32, tag);
  }
  STAMP(7);
  const rsrc_t rut = mkrsrc(p.ws + WS_UT, D * L * 2);
  u32x4 uv[4];
#pragma unroll
  for (int j = 0; j < 4; ++j) uv[j] = bld128<SC1>(rut, d * 4096 + 1024 * j + 16 * lane, 0);
#pragma unroll
  for (int j = 0; j < 4; ++j) lds_w128(ub + 1024 * j + 16 * lane, uv[j]);
  fence_c();
  STAMP(8);
  {
    u32x2 pv[3], nx[3]; u32x4 cu[3];
#pragma unroll
    for (int it = 0; it < 3; ++it) {
      const u32 a = ub + 1024 * it + 16 * lane;
      pv[it] = lds_r64(a - 8); cu[it] = lds_r128(a); nx[it] = lds_r64(a + 16);
    }
    const u32x2 u0 = lds_r64(ub);
#pragma unroll
    for (int it = 0; it < 3; ++it) {
      float v[16];
      v[0] = bflo(pv[it].x); v[1] = bfhi(pv[it].x); v[2] = bflo(pv[it].y); v[3] = bfhi(pv[it].y);
#pragma unroll
      for (int e = 0; e < 4; ++e) { v[4 + 2 * e] = bflo(cu[it][e]); v[5 + 2 * e] = bfhi(cu[it][e]); }
      v[12] = bflo(nx[it].x); v[13] = bfhi(nx[it].x); v[14] = bflo(nx[it].y); v[15] = bfhi(nx[it].y);
      float w3[8], w5[8], w7[8], w9[8];
#pragma unroll
      for (int e = 0; e < 8; ++e) {
        w3[e] = v[4 + e] + (v[3 + e] + v[5 + e]);
        w5[e] = w3[e] + (v[2 + e] + v[6 + e]);
        if (it < 2) { w7[e] = w5[e] + (v[1 + e] + v[7 + e]); w9[e] = w7[e] + (v[e] + v[8 + e]); }
      }
      const u32 o = 64 + 1024 * it + 16 * lane;
      lds_w128(cv + CV_W5 + o, u32x4{pk_bf16(w5[0], w5[1]), pk_bf16(w5[2], w5[3]), pk_bf16(w5[4], w5[5]), pk_bf16(w5[6], w5[7])});
      lds_w128(cv + CV_W3 + o, u32x4{pk_bf16(w3[0], w3[1]), pk_bf16(w3[2], w3[3]), pk_bf16(w3[4], w3[5]), pk_bf16(w3[6], w3[7])});
      if (it < 2) {
        lds_w128(cv + CV_W9 + o, u32x4{pk_bf16(w9[0], w9[1]), pk_bf16(w9[2], w9[3]), pk_bf16(w9[4], w9[5]), pk_bf16(w9[6], w9[7])});
        lds_w128(cv + CV_W7 + o, u32x4{pk_bf16(w7[0], w7[1]), pk_bf16(w7[2], w7[3]), pk_bf16(w7[4], w7[5]), pk_bf16(w7[6], w7[7])});
      }
    }
    fence_c();
    const float a0 = bflo(u0.x), a1 = a0 + bfhi(u0.x), a2 = a1 + bflo(u0.y), a3 = a2 + bfhi(u0.y);
    if (lane == 0) {
      lds_w64(cv + CV_W9 + 64 - 8, u32x2{pk_bf16(a0, a1), pk_bf16(a2, a3)});
      lds_w64(cv + CV_W7 + 64 - 8, u32x2{pk_bf16(0.f, a0), pk_bf16(a1, a2)});
      lds_w64(cv + CV_W5 + 64 - 8, u32x2{0u, pk_bf16(a0, a1)});
      lds_w64(cv + CV_W3 + 64 - 8, u32x2{0u, pk_bf16(0.f, a0)});
    }
  }
  fence_c();
  STAMP(9);
  f32x4 acc[8];
#pragma unroll
  for (int t = 0; t < 8; ++t) acc[t] = f32x4{0, 0, 0, 0};
  {
    const u32 bb = ub + 2 * (16 * r16 - 16 * (g >> 1) + 8 * (g & 1));
    const u32 kc = (*(LAS u16*)(uintptr_t)(ke + 2 * (832 + 144 - 64))) ^ 0x8000u;
    const int idx0 = 1008 + 16 * (g >> 1) - 8 * (g & 1) + r16;
    u32x4 C;
#pragma unroll
    for (int e = 0; e < 4; ++e) {
      const int i0 = idx0 - 2 * e, i1 = i0 - 1;
      const u32 lo = (i0 == 1020 || i0 == 1021) ? kc : 0u, hi = (i1 == 1020 || i1 == 1021) ? kc : 0u;
      C[e] = lo | (hi << 16);
    }
    constexpr int PF = 6;
    u32x4 B[PF];
#pragma unroll
    for (int j = 0; j < PF; ++j) B[j] = lds_r128(bb + 64 * (j - 7));
    SB();
#pragma unroll
    for (int z = -7; z <= 56; ++z) {
      const int sl = (z + 7) % PF;
#pragma unroll
      for (int t = 0; t < 8; ++t) {
        const int k = 8 * t - z;
        const int kmax = t == 0 ? 7 : (t == 1 ? 15 : 16);
        if (k >= 0 && k <= kmax) acc[t] = mfma16(A[k], B[sl], acc[t]);
      }
      if (z + PF <= 56) B[sl] = lds_r128(bb + 64 * (z + PF));
      else if (z + PF - 57 < 5) B[sl] = lds_r128(bb + 2 * (256 * (z + PF - 57 + 3) - 16 * 63));
      SB();
    }
#pragma unroll
    for (int t = 3; t < 8; ++t) acc[t] = mfma16(C, B[(57 + t - 3 + 7) % PF], acc[t]);
  }
  STAMP(10);
  const u32 zero_a = cv + CV_U + 8 * (lane & 3);
  const u32 q = r16 >> 2, pp = lane & 3;
  {
    u32x4 T4[2][3], T8[5];
#pragma unroll
    for (int pr = 0; pr < 2; ++pr)
#pragma unroll
      for (int dl = 0; dl < 3; ++dl) {
        const u32 a = kpar + 2 * (576 + 64 * (2 * pr + (g >> 1)) + 80 - 16 * (dl + 2) - r16 + 4 * (g & 1));
        T4[pr][dl] = u32x4{lds_r32(a), lds_r32(a + 4), lds_r32(a + 16), lds_r32(a + 20)};
      }
#pragma unroll
    for (int dl = 0; dl < 5; ++dl) {
      const u32 a = kpar + 2 * (832 + 96 * (g >> 1) + 144 - 16 * (dl + 4) - r16 + 4 * (g & 1));
      T8[dl] = u32x4{lds_r32(a), lds_r32(a + 4), lds_r32(a + 16), lds_r32(a + 20)};
    }
    const u32 wb4 = cv + ((g >> 1) ? CV_W3 - 8 : CV_W5) + 64 + 2 * (16 * (4 * (g & 1) + q) + 4 * pp);
    const u32 wb8 = cv + ((g >> 1) ? CV_W7 - 16 : CV_W9) + 64 + 2 * (16 * (4 * (g & 1) + q) + 4 * pp);
    const bool edge = (g & 1) && q == 3;
    u32x4 A4[2][7], A8[5];
#pragma unroll
    for (int pr = 0; pr < 2; ++pr)
#pragma unroll
      for (int be = -1; be <= 5; ++be) {
        if (be == -1) {
          if (pr == 0) { const u32x2 hi = lds_tr(edge ? wb4 - 512 + 256 : zero_a); A4[0][0] = u32x4{0u, 0u, hi.x, hi.y}; }
        } else {
          const u32 a = wb4 + 512 * be - 16 * pr;
          A4[pr][be + 1] = cat(lds_tr(a), lds_tr(a + 256));
        }
      }
#pragma unroll
    for (int be = -1; be <= 3; ++be) {
      if (be == -1) { const u32x2 hi = lds_tr(edge ? wb8 - 512 + 256 : zero_a); A8[0] = u32x4{0u, 0u, hi.x, hi.y}; }
      else { const u32 a = wb8 + 512 * be; A8[be + 1] = cat(lds_tr(a), lds_tr(a + 256)); }
    }
#pragma unroll
    for (int be = -1; be <= 5; ++be)
#pragma unroll
      for (int pr = 0; pr < 2; ++pr) {
        if (be == -1 && pr == 1) continue;
#pragma unroll
        for (int dl = 0; dl < 3; ++dl) {
          const int t = be + dl + 2;
          if (t >= 1 && t <= 7) acc[t] = mfma16(A4[pr][be + 1], T4[pr][dl], acc[t]);
        }
      }
#pragma unroll
    for (int be = -1; be <= 3; ++be)
#pragma unroll
      for (int dl = 0; dl < 5; ++dl) {
        const int t = be + dl + 4;
        if (t >= 3 && t <= 7) acc[t] = mfma16(A8[be + 1], T8[dl], acc[t]);
      }
  }
  STAMP(11);
  fence_c();
  const float ds = bf2f(dskip);
  const u32 ob = cv + CV_W5;
  u32x2 uu[8];
#pragma unroll
  for (int t = 0; t < 8; ++t) uu[t] = lds_r64(ub + 2 * (256 * t + 16 * r16 + 4 * g));
#pragma unroll
  for (int t = 0; t < 8; ++t) {
    const u32 y01 = pk_bf16(acc[t][0], acc[t][1]), y23 = pk_bf16(acc[t][2], acc[t][3]);
    const u32 s01 = pk_bf16(bflo(uu[t].x) * ds, bfhi(uu[t].x) * ds), s23 = pk_bf16(bflo(uu[t].y) * ds, bfhi(uu[t].y) * ds);
    lds_w64(ob + 2 * (256 * t + 16 * r16 + 4 * g), u32x2{pk_bf16(bflo(y01) + bflo(s01), bfhi(y01) + bfhi(s01)), pk_bf16(bflo(y23) + bflo(s23), bfhi(y23) + bfhi(s23))});
  }
  fence_c();
  const rsrc_t rat = mkrsrc(p.ws + WS_AT, D * L * 2);
#pragma unroll
  for (int j = 0; j < 4; ++j) bst128<SC1>(rat, lds_r128(ob + 1024 * j + 16 * lane), d * 4096 + 1024 * j + 16 * lane, 0);
  const u64 tag = get_tag(lds0);
  wait_vm0();
  if (lane == 0) gst64(p.ws, WS_FA + 8 * d, tag);
}

#define WPF 6
constexpr int WP = WPF;
template <int S0, int S1>
DEVI void gemm2_prefetch(const Params& p, int kw, int lane, int j, u32x4 (&wf)[WP][5]) {
  const rsrc_t rw = mkrsrc(p.ws + WS_W2F, 8 * 48 * 5 * 1024);
  const u32 vo = lane * 16, wb = (j * 48 + 12 * kw) * 5120;
#pragma unroll
  for (int s = S0; s < S1; ++s)
#pragma unroll
    for (int n = 0; n < 5; ++n) wf[s][n] = bld128<SC1>(rw, vo + 1024 * (n & 3), wb + s * 5120 + (n >> 2) * 4096);
}
DEVI void gemm2_main(const Params& p, u32 lds0, int kw, int lane, int m2, int j, u32x4 (&wf)[WP][5], f32x4 (&acc)[4][5]) {
  const rsrc_t rh = mkrsrc(p.ws + WS_HF, 32 * 48 * 4 * 1024), rw = mkrsrc(p.ws + WS_W2F, 8 * 48 * 5 * 1024);
  const u32 vo = lane * 16;
  const u32 hb = (m2 * 48 + 12 * kw) * 4096, wb = (j * 48 + 12 * kw) * 5120;
  constexpr int DP = 2;
  u32x4 hf[DP][4];
#pragma unroll
  for (int s = 0; s < DP; ++s)
#pragma unroll
    for (int m = 0; m < 4; ++m) hf[s][m] = bld128<SC1>(rh, vo + 1024 * m, hb + s * 4096);
#pragma unroll
  for (int s = 0; s < 12; ++s) {
    const int sl = s % DP, sw = s % WP;
    if (s == G2SIG && kw == 0) sy_set(lds0 + LDS_SYNC + 4 * SY_G2, 1, lane);
#pragma unroll
    for (int n = 0; n < 5; ++n) {
#pragma unroll
      for (int m = 0; m < 4; ++m) {
        if (s == 0) mfma16z(acc[m][n], wf[sw][n], hf[sl][m]); else mfma16t(acc[m][n], wf[sw][n], hf[sl][m]);
        if (n == 4 && s + DP < 12) { SB(); hf[sl][m] = bld128<SC1>(rh, vo + 1024 * m, hb + (s + DP) * 4096); SB(); }
      }
      if (s + WP < 12) { SB(); wf[sw][n] = bld128<SC1>(rw, vo + 1024 * (n & 3), wb + (s + WP) * 5120 + (n >> 2) * 4096); }
      SB();
    }
  }
  asm volatile("s_nop 7\n\ts_nop 3" : "+v"(acc[0][4]), "+v"(acc[1][4]), "+v"(acc[2][4]), "+v"(acc[3][4]), "+v"(acc[0][3]), "+v"(acc[1][3]), "+v"(acc[2][3]), "+v"(acc[3][3]) :: "memory");
  asm volatile("" : "+v"(acc[0][0]), "+v"(acc[1][0]), "+v"(acc[2][0]), "+v"(acc[3][0]), "+v"(acc[0][1]), "+v"(acc[1][1]), "+v"(acc[2][1]), "+v"(acc[3][1]), "+v"(acc[0][2]), "+v"(acc[1][2]), "+v"(acc[2][2]), "+v"(acc[3][2]) :: "memory");
}

__global__ __launch_bounds__(512, 2) void fused_kernel(Params p) {
  extern __shared__ __attribute__((aligned(16))) char smem[];
  const u32 lds0 = (u32)(uintptr_t)(LAS char*)smem;
  const int b = blockIdx.x, lane = threadIdx.x & 63, w = rfl(threadIdx.x >> 6);
  const int x = b & 7, i = b >> 3;
  const u32 sy = lds0 + LDS_SYNC;
  const int mt = 4 * (x >> 1) + (i >> 3), nt = 8 * (x & 1) + (i & 7);
  const int m2 = 4 * x + (i >> 3), j = i & 7;
  STAMP(0);
  if (threadIdx.x < 16) lds_w32(sy + 4 * threadIdx.x, 0);
  wait_lgkm0();
  __builtin_amdgcn_s_barrier();
  fence_c();
  f32x4 acc[4][5];
  if (w < 4) {
    __builtin_amdgcn_s_setprio(PRIO_G);
    gemm1_wave(p, lds0, w, lane, mt, nt, b STAMP_PASS);
    STAMP(5);
    u32x4 g2[WP][5];
    const u64 tag = get_tag(lds0);
    sy_wait_ge(sy + 4 * SY_AUX, 1);
    gemm2_prefetch<0, 1>(p, w, lane, j, g2);
    SB();
    asm volatile("s_waitcnt vmcnt(5)" ::: "memory");
    if (lane == 0) gst64(p.ws, WS_FH + 8 * (((2 * mt + (w >> 1)) * 16 + nt) * 2 + (w & 1)), tag);
    STAMP(7);
    SB();
    gemm2_prefetch<1, WP>(p, w, lane, j, g2);
    SB();
    poll_flags(p.ws, WS_FH + 8 * (m2 * 32 + 8 * w + (lane & 7)), lane < 8, tag);
    STAMP(6);
    gemm2_main(p, lds0, w, lane, m2, j, g2, acc);
    STAMP(13);
#pragma unroll
    for (int m = 0; m < 4; ++m) {
      if (m == w) continue;
      const u32 slot = (w - m - 1) & 3;
#pragma unroll
      for (int n = 0; n < 5; ++n) lds_w128(lds0 + LDS_RED + ((m * 3 + slot) * 5 + n) * 1024 + 16 * lane, __builtin_bit_cast(u32x4, acc[m][n]));
    }
  } else if (w == 7) {
    __builtin_amdgcn_s_setprio(3);
    gemm1_loader(p, lds0, lane, mt, nt, ROT1 ? ((i & 7) + 3 * (i >> 3)) % 10 : 0 STAMP_PASS);
    STAMP(5);
    const u64 tag = get_tag(lds0);
    {
      const int ca = (80 * j) / 128, cb = (80 * j + 79) / 128;
      poll_flags(p.ws, WS_FW + 8 * (ca * 64 + lane), lane < 48, tag);
      if (cb != ca) poll_flags(p.ws, WS_FW + 8 * (cb * 64 + lane), lane < 48, tag);
    }
    sy_set(sy + 4 * SY_AUX, 1, lane);
    STAMP(6);
    sy_wait_ge(sy + 4 * SY_G2, 1);
    STAMP(7);
    poll_flags(p.ws, WS_FG + 8 * (m2 * 8 + (lane & 7)), lane < 8, tag);
    poll_flags<FA_SLEEP>(p.ws, WS_FA + 8 * (80 * j + lane), true, tag);
    poll_flags<FA_SLEEP>(p.ws, WS_FA + 8 * (80 * j + 64 + (lane & 15)), lane < 16, tag);
    STAMP(8);
    const rsrc_t rat = mkrsrc(p.ws + WS_AT, D * L * 2);
    u32x4 av[10];
#pragma unroll
    for (int jj = 0; jj < 10; ++jj) {
      const u32 id = 64 * jj + lane, row = id >> 3, c = id & 7;
      av[jj] = bld128<SC1>(rat, (80 * j + row) * 4096 + 128 * m2 + 16 * c, 0);
    }
    const float gv = __hip_atomic_load((GAS float*)(p.ws + WS_G) + 64 * m2 + lane, RLX_AGENT);
#pragma unroll
    for (int jj = 0; jj < 10; ++jj) {
      const u32 id = 64 * jj + lane, row = id >> 3, c = id & 7;
      lds_w128(lds0 + LDS_ATILE + row * 160 + 16 * c, av[jj]);
    }
    lds_w32(lds0 + LDS_GT + 4 * lane, __float_as_uint(gv));
    STAMP(13);
  } else {
    __builtin_amdgcn_s_setprio(PRIO_K);
    const int xw = w - 4, nch = b < 128 ? 3 : 2, d0 = b < 128 ? 3 * b : 384 + 2 * (b - 128);
    if (xw < nch) {
      const u32 cv = lds0 + LDS_CONV + xw * CV_SZ;
      kgen_wave(p, lds0, cv, lane, d0 + xw, xw == 0 STAMP_PASS);
      STAMP(5);
      conv_wave(p, lds0, cv, lane, d0 + xw STAMP_PASS);
      STAMP(13);
    }
  }
  wait_lgkm0();
  __builtin_amdgcn_s_barrier();
  fence_c();
  STAMP(14);
  if (w < 4) {
    const u32 g = lane >> 4, r16 = lane & 15;
    f32x4 own[5];
#pragma unroll
    for (int m = 0; m < 4; ++m)
      if (m == w) {
#pragma unroll
        for (int n = 0; n < 5; ++n) own[n] = acc[m][n];
      }
    const int t = 64 * m2 + 16 * w + r16;
    const float gv = __uint_as_float(lds_r32(lds0 + LDS_GT + 4 * (16 * w + r16)));
    const float og = rbf(1.f - gv);
    const rsrc_t ro = mkrsrc(p.out, L * D * 2);
#pragma unroll
    for (int n = 0; n < 5; ++n) {
#pragma unroll
      for (int s = 0; s < 3; ++s) own[n] += __builtin_bit_cast(f32x4, lds_r128(lds0 + LDS_RED + ((w * 3 + s) * 5 + n) * 1024 + 16 * lane));
      const u32x2 av = lds_tr(lds0 + LDS_ATILE + (16 * n + 4 * g + (r16 >> 2)) * 160 + 32 * w + 8 * (lane & 3));
      const u32 b01 = pk_bf16(own[n][0], own[n][1]), b23 = pk_bf16(own[n][2], own[n][3]);
      const u32 ga01 = pk_bf16(gv * bflo(av.x), gv * bfhi(av.x)), ga23 = pk_bf16(gv * bflo(av.y), gv * bfhi(av.y));
      const u32 gb01 = pk_bf16(og * bflo(b01), og * bfhi(b01)), gb23 = pk_bf16(og * bflo(b23), og * bfhi(b23));
      const u32x2 o = {pk_bf16(bflo(ga01) + bflo(gb01), bfhi(ga01) + bfhi(gb01)), pk_bf16(bflo(ga23) + bflo(gb23), bfhi(ga23) + bfhi(gb23))};
      bst64<0>(ro, o, t * 1280 + (80 * j + 16 * n + 4 * g) * 2, 0);
    }
    if (b == 0 && w == 0) { const u64 tag = get_tag(lds0); if (lane == 0) gst64(p.ws, WS_EP, tag); }
  }
}

extern "C" void kernel_launch(void* const* d_in, const int* in_sizes, int n_in, void* d_out, int out_size, void* d_ws, size_t ws_size,
                              hipStream_t stream) {
  Params p;
  p.u = (const u16*)d_in[0]; p.Are = (const u16*)d_in[1]; p.Aim = (const u16*)d_in[2]; p.Bre = (const u16*)d_in[3];
  p.Bim = (const u16*)d_in[4]; p.Cre = (const u16*)d_in[5]; p.Cim = (const u16*)d_in[6]; p.logdt = (const u16*)d_in[7];
  p.Dskip = (const u16*)d_in[8]; p.w1 = (const u16*)d_in[10]; p.w2 = (const u16*)d_in[11]; p.gw = (const u16*)d_in[12];
  p.out = (u16*)d_out; p.ws = (char*)d_ws;
  (void)hipFuncSetAttribute((const void*)fused_kernel, hipFuncAttributeMaxDynamicSharedMemorySize, LDS_TOTAL);
  fused_kernel<<<256, 512, LDS_TOTAL, stream>>>(p);
}
```

```cpp
#include <hip/hip_runtime.h>
#include <stdint.h>

typedef unsigned short u16;
typedef unsigned u32;
typedef unsigned long long u64;
typedef float f32x4 __attribute__((ext_vector_type(4)));
typedef float f32x2 __attribute__((ext_vector_type(2)));
typedef __bf16 bf16x8 __attribute__((ext_vector_type(8)));
typedef __bf16 bf16x2 __attribute__((ext_vector_type(2)));
typedef short s16x4 __attribute__((ext_vector_type(4)));
typedef unsigned u32x4 __attribute__((ext_vector_type(4)));
typedef unsigned u32x2 __attribute__((ext_vector_type(2)));
#define LAS __attribute__((address_space(3)))
#define GAS __attribute__((address_space(1)))
#define DEVI __device__ __forceinline__
#define SB() __builtin_amdgcn_sched_barrier(0)
#define RLX_AGENT __ATOMIC_RELAXED, __HIP_MEMORY_SCOPE_AGENT

constexpr int L = 2048, D = 640, NM = 192, F = 1536;
#define ROT1 1
#define G2SIG 7
#define PRIO_K 0
#define PRIO_G 2
#define FA_SLEEP 8
#define FU_SLEEP 16
#define PUBC 2
#define O_KPRE 0
#define O_CPRIO 1
#define O_EARLYPOLL 1
#define HPOLL 6
#define PRIO_L 1
#define O_CODEPF 1
#define O_PF 0
#define PF_SLEEP 3
#define XPOL ""
#define WPOL ""
#define OUTPOL 0
#define O_EARLYT 2
#define PUBU 1
#define O_WIDEOUT 1
#define O_TLB 0
#define NTP 1
#define NTM 2
#define LSLEEP 0
#define PREBAR 0
#define PRIO_NEAR 0
constexpr int SC1 = 16;

constexpr u32 WS_HF   = 0;
constexpr u32 WS_W2F  = WS_HF + 32 * 48 * 4 * 1024;
constexpr u32 WS_UT   = WS_W2F + 8 * 48 * 5 * 1024;
constexpr u32 WS_AT   = WS_UT + D * L * 2;
constexpr u32 WS_KIMG = WS_AT + D * L * 2;
constexpr u32 WS_G    = WS_KIMG + D * 2048;
constexpr u32 WS_EP   = WS_G + L * 4;
#define FS 128
constexpr u32 WS_FU   = WS_EP + 256;
constexpr u32 WS_FW   = WS_FU + 8 * 32 * FS;
constexpr u32 WS_FG   = WS_FW + 5 * 64 * FS;
constexpr u32 WS_FH   = WS_FG + 32 * 8 * FS;
constexpr u32 WS_FA   = WS_FH + 32 * 32 * FS;
constexpr u32 WS_END  = WS_FA + 640 * FS;

constexpr u32 STG = 28672;
constexpr u32 LDS_RED = 0;
constexpr u32 LDS_ATILE = 61440;
constexpr u32 LDS_GT = LDS_ATILE + 12800;
constexpr u32 LDS_CONV = 3 * STG;
constexpr u32 CV_U = 0;
constexpr u32 CV_W5 = 4608;
constexpr u32 CV_W3 = CV_W5 + 3136;
constexpr u32 CV_W9 = CV_W3 + 3136;
constexpr u32 CV_W7 = CV_W9 + 2112;
constexpr u32 CV_KE = CV_W7 + 2112;
constexpr u32 CV_KO = CV_KE + 2048 + 16;
constexpr u32 CV_SZ = CV_KO + 2048;
constexpr u32 CV_TAB = 0;
constexpr u32 LDS_SYNC = LDS_CONV + 3 * CV_SZ;
constexpr u32 SY_DONE = 0  , SY_FULL = 4, SY_AUX = 5, SY_H = 6, SY_MISC = 7, SY_TAG = 8  , SY_TAGV = 10, SY_SCR = 11, SY_G2 = 12, SY_C0 = 13;
constexpr u32 LDS_DBG = LDS_SYNC + 64;
constexpr u32 LDS_JUNK = LDS_SYNC + 64 + 1024;
constexpr u32 LDS_TOTAL = LDS_JUNK + 256;
constexpr u32 SPIN_CAP = 1u << 20, POLL_CAP = 1u << 16;

struct Params {
  const u16 *u, *Are, *Aim, *Bre, *Bim, *Cre, *Cim, *logdt, *Dskip, *w1, *w2, *gw;
  u16* out;
  char* ws;
};

#define STAMP(k) do {} while (0)
#define STAMP_ARG
#define TRACE(i) do {} while (0)
#define STAMP_PASS

DEVI float bf2f(u32 x16) { return __uint_as_float(x16 << 16); }
DEVI float bflo(u32 x) { return __uint_as_float(x << 16); }
DEVI float bfhi(u32 x) { return __uint_as_float(x & 0xffff0000u); }
DEVI u32 pk_bf16(float lo, float hi) {
  bf16x2 v = {(__bf16)lo, (__bf16)hi};
  return __builtin_bit_cast(u32, v);
}
DEVI float rbf(float x) { return bflo(pk_bf16(x, x) & 0xffffu); }
DEVI float dot2bf(u32 a, u32 b, float c) { return __builtin_amdgcn_fdot2_f32_bf16(__builtin_bit_cast(bf16x2, a), __builtin_bit_cast(bf16x2, b), c, false); }
DEVI f32x4 mfma16(u32x4 a, u32x4 b, f32x4 c) {
  return __builtin_amdgcn_mfma_f32_16x16x32_bf16(__builtin_bit_cast(bf16x8, a), __builtin_bit_cast(bf16x8, b), c, 0, 0, 0);
}
DEVI void mfma16t(f32x4& acc, u32x4 a, u32x4 b) { asm volatile("v_mfma_f32_16x16x32_bf16 %0, %1, %2, %0" : "+v"(acc) : "v"(a), "v"(b)); }
DEVI void mfma16z(f32x4& acc, u32x4 a, u32x4 b) { asm volatile("v_mfma_f32_16x16x32_bf16 %0, %1, %2, 0" : "=&v"(acc) : "v"(a), "v"(b)); }
DEVI u32x4 lds_r128(u32 a) { return *(LAS u32x4*)(uintptr_t)a; }
DEVI u32x2 lds_r64(u32 a) { return *(LAS u32x2*)(uintptr_t)a; }
DEVI u32 lds_r32(u32 a) { return *(LAS u32*)(uintptr_t)a; }
DEVI void lds_w128(u32 a, u32x4 v) { *(LAS u32x4*)(uintptr_t)a = v; }
DEVI void lds_w64(u32 a, u32x2 v) { *(LAS u32x2*)(uintptr_t)a = v; }
DEVI void lds_w32(u32 a, u32 v) { *(LAS u32*)(uintptr_t)a = v; }
DEVI void lds_w16(u32 a, u32 v) { *(LAS u16*)(uintptr_t)a = (u16)v; }
DEVI u32 lds_vr32(u32 a) { return *(volatile LAS u32*)(uintptr_t)a; }
DEVI u32x4 lds_vr128(u32 a) { return *(volatile LAS u32x4*)(uintptr_t)a; }
DEVI void lds_vw32(u32 a, u32 v) { *(volatile LAS u32*)(uintptr_t)a = v; }
DEVI u32x2 lds_tr(u32 a) { return __builtin_bit_cast(u32x2, __builtin_amdgcn_ds_read_tr16_b64_v4i16((LAS s16x4*)(uintptr_t)a)); }
DEVI u32x4 cat(u32x2 a, u32x2 b) { return u32x4{a.x, a.y, b.x, b.y}; }
typedef __amdgpu_buffer_rsrc_t rsrc_t;
DEVI rsrc_t mkrsrc(const void* p, u32 bytes) { return __builtin_amdgcn_make_buffer_rsrc((void*)p, 0, bytes, 0x00020000); }
template <int AUX> DEVI u32x4 bld128(rsrc_t r, u32 v, u32 s) { return __builtin_amdgcn_raw_buffer_load_b128(r, v, s, AUX); }
template <int AUX> DEVI void bst128(rsrc_t r, u32x4 d, u32 v, u32 s) { __builtin_amdgcn_raw_buffer_store_b128(d, r, v + s, 0, AUX); }
template <int AUX> DEVI void bst64(rsrc_t r, u32x2 d, u32 v, u32 s) { __builtin_amdgcn_raw_buffer_store_b64(d, r, v + s, 0, AUX); }
DEVI u32 rfl(u32 x) { return __builtin_amdgcn_readfirstlane(x); }
DEVI void wait_lgkm0() { asm volatile("s_waitcnt lgkmcnt(0)" ::: "memory"); }
DEVI void wait_vm0() { asm volatile("s_waitcnt vmcnt(0)" ::: "memory"); }
DEVI void fence_c() { asm volatile("" ::: "memory"); }
DEVI u32 min4(u32x4 f) { return min(min(f.x, f.y), min(f.z, f.w)); }
DEVI void sy_set(u32 a, u32 v, int lane) { if (lane == 0) lds_vw32(a, v); }
DEVI void sy_wait_ge(u32 a, u32 v) { u32 n = 0; while (rfl(lds_vr32(a)) < v && ++n < SPIN_CAP) {} fence_c(); }
DEVI u64 gld64(const char* ws, u32 off) { return __hip_atomic_load((GAS u64*)(ws + off), RLX_AGENT); }
DEVI void gst64(char* ws, u32 off, u64 v) { __hip_atomic_store((GAS u64*)(ws + off), v, RLX_AGENT); }
DEVI u64 get_tag(u32 lds0) {
  sy_wait_ge(lds0 + LDS_SYNC + 4 * SY_TAGV, 1);
  const u32x2 t = *(volatile LAS u32x2*)(uintptr_t)(lds0 + LDS_SYNC + 4 * SY_TAG);
  return ((u64)rfl(t.y) << 32) | rfl(t.x);
}
template <int SLEEP = 4>
DEVI void poll_flags(const char* ws, u32 off, bool act, u64 tag) {
  for (u32 n = 0; n < POLL_CAP; ++n) {
    const u64 v = act ? gld64(ws, off) : tag;
    if (__all(v == tag)) break;
    __builtin_amdgcn_s_sleep(SLEEP);
  }
  fence_c();
}

DEVI void cmul2(f32x2& r0, f32x2 a0, f32x2 b0, f32x2& r1, f32x2 a1, f32x2 b1) {
  f32x2 t0, t1;
  asm("v_pk_mul_f32 %2, %4, %5 op_sel:[1,1] op_sel_hi:[1,0] neg_lo:[0,1]\n\t"
      "v_pk_mul_f32 %3, %6, %7 op_sel:[1,1] op_sel_hi:[1,0] neg_lo:[0,1]\n\t"
      "v_pk_fma_f32 %0, %4, %5, %2 op_sel:[0,0,0] op_sel_hi:[0,1,1]\n\t"
      "v_pk_fma_f32 %1, %6, %7, %3 op_sel:[0,0,0] op_sel_hi:[0,1,1]"
      : "=&v"(r0), "=&v"(r1), "=&v"(t0), "=&v"(t1) : "v"(a0), "v"(b0), "v"(a1), "v"(b1));
}
DEVI f32x2 cexp_pow(float ex, float rev, float m) {
  float e = __builtin_amdgcn_exp2f(ex * m);
  float fr = __builtin_amdgcn_fractf(rev * m);
  return f32x2{e * __builtin_amdgcn_cosf(fr), e * __builtin_amdgcn_sinf(fr)};
}
DEVI f32x2 conj(f32x2 a) { return f32x2{a.x, -a.y}; }
DEVI u32 pk2(f32x2 v) { return pk_bf16(v.x, v.y); }

struct T_ { static constexpr bool value = true; };
struct F_ { static constexpr bool value = false; };

DEVI void misc_load(const Params& p, int w, int lane, int b, u32x4 (&ra)[10], u32x4 (&rb)[10]) {
#pragma unroll
  for (int j = 0; j < 10; ++j) { ra[j] = u32x4{0, 0, 0, 0}; rb[j] = u32x4{0, 0, 0, 0}; }
  const int x = b & 7, i = b >> 3, rq = x >> 1, fh = x & 1;
  const u32 g = lane >> 4;
  const rsrc_t rx = mkrsrc(p.u, L * D * 2);
  if (w < 2) {
    const int tt = 8 * rq + (i >> 2), dtile = 4 * fh + (i & 3);
#pragma unroll
    for (int j = 0; j < 5; ++j) {
      const u32 id = 64 * j + lane, row = id / 10, c = id % 10;
      ra[j] = bld128<NTM>(rx, (64 * tt + 32 * w + row) * 1280 + 160 * dtile + 16 * c, 0);
    }
  } else if (w == 2) {
    const rsrc_t rw2 = mkrsrc(p.w2, F * D * 2);
    const int kap = b % 48, c5 = b / 48;
#pragma unroll
    for (int j = 0; j < 8; ++j) ra[j] = bld128<NTM>(rw2, (32 * kap + 4 * j + g) * 1280 + 256 * c5 + 16 * (lane & 15), 0);
  } else {
    const int grow = 512 * rq + 256 * fh + 8 * i + (lane >> 3);
    const rsrc_t rg = mkrsrc(p.gw, D * 2);
#pragma unroll
    for (int j = 0; j < 10; ++j) {
      ra[j] = bld128<NTM>(rx, grow * 1280 + 16 * ((lane & 7) + 8 * j), 0);
      rb[j] = bld128<0>(rg, 16 * ((lane & 7) + 8 * j), 0);
    }
  }
}
DEVI void misc_process(const Params& p, u32 lds0, int w, int lane, int b, const u32x4 (&ra)[10], const u32x4 (&rb)[10]) {
  const int x = b & 7, i = b >> 3, rq = x >> 1, fh = x & 1;
  const u32 g = lane >> 4, q = (lane & 15) >> 2, pp = lane & 3;
  if (w < 2) {
    const u32 ms = lds0 + LDS_CONV + w * CV_SZ + 9216;
    const int tt = 8 * rq + (i >> 2), dtile = 4 * fh + (i & 3);
#pragma unroll
    for (int j = 0; j < 5; ++j) {
      const u32 id = 64 * j + lane, row = id / 10, c = id % 10;
      lds_w128(ms + row * 176 + 16 * c, ra[j]);
    }
    fence_c();
    const rsrc_t rut = mkrsrc(p.ws + WS_UT, D * L * 2);
#pragma unroll
    for (int dtl = 0; dtl < 5; ++dtl) {
      const u32 a = ms + (8 * g + q) * 176 + 32 * dtl + 8 * pp;
      const u32x4 v = cat(lds_tr(a), lds_tr(a + 4 * 176));
      bst128<SC1>(rut, v, (80 * dtile + 16 * dtl + (lane & 15)) * 4096 + (64 * tt + 32 * w + 8 * g) * 2, 0);
    }
  } else if (w == 2) {
    if (b < 240) {
      const u32 ms = lds0 + LDS_CONV + 2 * CV_SZ + 9216;
      const rsrc_t rwf = mkrsrc(p.ws + WS_W2F, 8 * 48 * 5 * 1024);
      const int kap = b % 48, c5 = b / 48;
#pragma unroll
      for (int hf = 0; hf < 2; ++hf) {
        fence_c();
        if (((lane >> 3) & 1) == hf) {
#pragma unroll
          for (int j = 0; j < 8; ++j) lds_w128(ms + (4 * j + g) * 160 + 16 * (lane & 7), ra[j]);
        }
        fence_c();
#pragma unroll
        for (int T = 0; T < 4; ++T) {
          const u32 a = ms + (4 * g + q) * 160 + 32 * T + 8 * pp;
          const u32x4 v = cat(lds_tr(a), lds_tr(a + 16 * 160));
          const int Tg = 8 * c5 + 4 * hf + T, j = Tg / 5, nu = Tg % 5;
          bst128<SC1>(rwf, v, lane * 16, ((j * 48 + kap) * 5 + nu) * 1024);
        }
      }
    }
  } else {
    const int grow = 512 * rq + 256 * fh + 8 * i + (lane >> 3);
    float s0 = 0.f, s1 = 0.f;
#pragma unroll
    for (int j = 0; j < 10; ++j)
#pragma unroll
      for (int e = 0; e < 4; e += 2) {
        s0 = dot2bf(ra[j][e], rb[j][e], s0);
        s1 = dot2bf(ra[j][e + 1], rb[j][e + 1], s1);
      }
    float s = s0 + s1;
    s += __shfl_xor(s, 1); s += __shfl_xor(s, 2); s += __shfl_xor(s, 4);
    if ((lane & 7) == 0) {
      const float sb = rbf(s);
      const float gv = rbf(1.f / (1.f + __expf(-sb)));
      __hip_atomic_store((GAS float*)(p.ws + WS_G) + grow, gv, RLX_AGENT);
    }
  }
}

DEVI void gemm1_loader(const Params& p, u32 lds0, int lane, int mt, int nt, int rot STAMP_ARG) {
  const rsrc_t rx = mkrsrc(p.u, L * D * 2), rw = mkrsrc(p.w1, D * F * 2);
  const u32 vx = (u32)(128 * mt + (lane >> 3)) * 1280 + (((lane & 7) ^ (lane >> 3)) & 7) * 16;
  u32 vw[3];
#pragma unroll
  for (int q = 0; q < 3; ++q) {
    const u32 o = 1024 * q + 16 * lane, row = o / 192, pc = (o % 192) >> 4;
    vw[q] = row * 3072 + 192 * nt + 16 * (pc ^ (2 * ((row >> 3) & 1)));
  }
  const u32 sy = lds0 + LDS_SYNC;
  if (LSLEEP) __builtin_amdgcn_s_sleep(LSLEEP);
  u32 st = 0;
  int cc = rot;
  for (int k = 0; k < 10; ++k) {
    if (ROT1 == 2) cc = k < 8 ? (k & ~3) + ((k + rot) & 3) : 8 + ((k + rot) & 1);
    if (ROT1 == 3) cc = (k / 5) * 5 + (k % 5 + rot) % 5;
    if (ROT1 == 4) cc = (k & ~1) + ((k + rot) & 1);
    TRACE(4 * k);
    if (k >= 3) {
      u32 n = 0;
      while (rfl(min4(lds_vr128(sy + 4 * SY_DONE))) < (u32)(k - 2) && ++n < SPIN_CAP) {}
      fence_c();
    }
    TRACE(4 * k + 1);
    u32 m = lds0 + st, sx = cc * 128, sw = cc * 196608;
    asm volatile(
        "s_mov_b32 m0, %[m]\n\t"
        ".rept 14\n\t"
        "s_nop 0\n\t"
        "buffer_load_dwordx4 %[vx], %[rx], %[sx] offen" XPOL " lds\n\t"
        "s_add_u32 m0, m0, 0x400\n\t"
        "s_add_u32 %[sx], %[sx], 10240\n\t"
        ".endr"
        : [sx] "+s"(sx) : [m] "s"(m), [vx] "v"(vx), [rx] "s"(rx) : "memory", "scc");
    if (PREBAR && k == 0) {
      wait_lgkm0();
      __builtin_amdgcn_s_barrier();
      fence_c();
    }
    if (k >= 2) {
      asm volatile("s_waitcnt vmcnt(14)" ::: "memory");
      sy_set(sy + 4 * SY_FULL, k, lane);
    }
    m += 14 * 1024;
    asm volatile(
        "s_mov_b32 m0, %[m]\n\t"
        ".rept 2\n\t"
        "s_nop 0\n\t"
        "buffer_load_dwordx4 %[vx], %[rx], %[sx] offen" XPOL " lds\n\t"
        "s_add_u32 m0, m0, 0x400\n\t"
        "s_add_u32 %[sx], %[sx], 10240\n\t"
        ".endr\n\t"
        ".rept 4\n\t"
        "buffer_load_dwordx4 %[v0], %[rw], %[sw] offen" WPOL " lds\n\t"
        "s_add_u32 m0, m0, 0x400\n\t"
        "s_nop 0\n\t"
        "buffer_load_dwordx4 %[v1], %[rw], %[sw] offen" WPOL " lds\n\t"
        "s_add_u32 m0, m0, 0x400\n\t"
        "s_nop 0\n\t"
        "buffer_load_dwordx4 %[v2], %[rw], %[sw] offen" WPOL " lds\n\t"
        "s_add_u32 m0, m0, 0x400\n\t"
        "s_add_u32 %[sw], %[sw], 49152\n\t"
        ".endr"
        : [sx] "+s"(sx), [sw] "+s"(sw)
        : [m] "s"(m), [vx] "v"(vx), [v0] "v"(vw[0]), [v1] "v"(vw[1]), [v2] "v"(vw[2]), [rx] "s"(rx), [rw] "s"(rw)
        : "memory", "scc");
    if (k == 0) {
      sy_set(sy + 4 * SY_C0, 1, lane);
      wait_vm0();
      sy_set(sy + 4 * SY_FULL, 1, lane);
    }
    st = st == 2 * STG ? 0 : st + STG;
    cc = cc == 9 ? 0 : cc + 1;
  }
  wait_vm0();
  sy_set(sy + 4 * SY_FULL, 10, lane);
}

DEVI void gemm1_wave(const Params& p, u32 lds0, int w, int lane, int mt, int nt, int b STAMP_ARG) {
  u32x4 mra[10], mrb[10];
  if (O_PF && w == 0) {
    const int x = b & 7, i = b >> 3;
    const u32 row = 160 * (x >> 1) + 5 * i + lane / 12, ln = lane % 12;
    const rsrc_t rw = mkrsrc(p.w1, D * F * 2);
    if (lane < 60)
      asm volatile("s_mov_b32 m0, %0\n\ts_nop 0\n\tbuffer_load_dword %1, %2, 0 offen lds" :: "s"(lds0 + LDS_JUNK), "v"(row * 3072 + 1536 * (x & 1) + 128 * ln), "s"(rw) : "memory");
  }
  if (!(O_PF && w == 3) && !(O_EARLYT && w < 2)) sy_wait_ge(lds0 + LDS_SYNC + 4 * SY_C0, 1);
  misc_load(p, w, lane, b, mra, mrb);
  if (O_EARLYT == 1 && w < 2) {
    misc_process(p, lds0, w, lane, b, mra, mrb);
    wait_lgkm0();
    if (lane == 0) __hip_atomic_fetch_add((LAS u32*)(uintptr_t)(lds0 + LDS_SYNC + 4 * SY_SCR), 1u, __ATOMIC_RELAXED, __HIP_MEMORY_SCOPE_WORKGROUP);
  }
  const u32 g = lane >> 4, r16 = lane & 15;
  const u32 sy = lds0 + LDS_SYNC;
  const u32 xb0 = lds0 + (32 * w + r16) * 128 + 16 * (g ^ (lane & 7));
  const u32 xb1 = lds0 + (32 * w + r16) * 128 + 16 * ((4 + g) ^ (lane & 7));
  const u32 wrow = lds0 + 16384 + (8 * g + (r16 >> 2)) * 192 + 16 * ((lane & 3) >> 1) + 8 * (lane & 1);
  const u32 wbe = wrow + 32 * (g & 1), wbo = wrow - 32 * (g & 1);
  f32x4 acc[6][2];
#pragma unroll
  for (int a = 0; a < 6; ++a) { acc[a][0] = f32x4{0, 0, 0, 0}; acc[a][1] = f32x4{0, 0, 0, 0}; }
  u32x4 xa[2], wa[6], xb[2], wb[6];
  auto rd = [&](int j, u32 st, int ks, u32x4 (&X)[2], u32x4 (&W)[6]) {
    if (j < 2) X[j] = lds_r128((ks ? xb1 : xb0) + st + 2048 * j);
    else {
      const int a = (j - 2) >> 1, h = (j - 2) & 1;
      const u32x2 v = lds_tr(((a & 1) ? wbo : wbe) + st + 32 * a + ks * 32 * 192 + h * 4 * 192);
      if (h == 0) { W[a].x = v.x; W[a].y = v.y; } else { W[a].z = v.x; W[a].w = v.y; }
    }
  };
  auto chunk = [&](int c, u32 st, u32 stn, auto LAST) {
    constexpr bool last = decltype(LAST)::value;
#pragma unroll
    for (int m = 0; m < 12; ++m) {
      acc[m >> 1][m & 1] = mfma16(wa[m >> 1], xa[m & 1], acc[m >> 1][m & 1]);
      rd(m, st, 1, xb, wb);
      if (m >= 10) rd(m + 2, st, 1, xb, wb);
      SB();
    }
    wait_lgkm0();
    u32 f = 0;
    if (!last) { sy_set(sy + 4 * SY_DONE + 4 * w, c + 1, lane); f = lds_vr32(sy + 4 * SY_FULL); }
    SB();
#pragma unroll
    for (int m = 0; m < 4; ++m) acc[m >> 1][m & 1] = mfma16(wb[m >> 1], xb[m & 1], acc[m >> 1][m & 1]);
    SB();
    if (!last) {
      u32 n = 0;
      if (w == 0) TRACE(64 + 2 * c);
      while (rfl(f) < (u32)(c + 2) && ++n < SPIN_CAP) f = lds_vr32(sy + 4 * SY_FULL);
      fence_c();
      if (w == 0) TRACE(64 + 2 * c + 1);
    }
#pragma unroll
    for (int m = 4; m < 12; ++m) {
      if (!last && m < 11) { rd(2 * (m - 4), stn, 0, xa, wa); rd(2 * (m - 4) + 1, stn, 0, xa, wa); }
      acc[m >> 1][m & 1] = mfma16(wb[m >> 1], xb[m & 1], acc[m >> 1][m & 1]);
      SB();
    }
  };
  sy_wait_ge(sy + 4 * SY_FULL, 1);
  STAMP(2);
#pragma unroll
  for (int j = 0; j < 14; ++j) rd(j, 0, 0, xa, wa);
  u32 st = 0;
  for (int c = 0; c < 9; ++c) {
    const u32 stn = st == 2 * STG ? 0 : st + STG;
    chunk(c, st, stn, F_{});
    st = stn;
    if (c == 0) {
      STAMP(1);
      if (!(O_EARLYT == 1 && w < 2)) {
        misc_process(p, lds0, w, lane, b, mra, mrb);
        wait_lgkm0();
        if (lane == 0) __hip_atomic_fetch_add((LAS u32*)(uintptr_t)(sy + 4 * SY_SCR), 1u, __ATOMIC_RELAXED, __HIP_MEMORY_SCOPE_WORKGROUP);
      }
    }
    if (c == (w < 2 ? (O_EARLYT ? PUBU : PUBC) : PUBC)) {
      wait_vm0();
      const int x = b & 7, i = b >> 3;
      bool pub = true;
      if (w < 2) {
        u32 old = 0;
        if (lane == 0) old = __hip_atomic_fetch_add((LAS u32*)(uintptr_t)(sy + 4 * SY_MISC), 1u, __ATOMIC_RELAXED, __HIP_MEMORY_SCOPE_WORKGROUP);
        pub = rfl(old) == 1;
      }
      if (pub && (w != 2 || b < 240)) {
        const u64 tag = get_tag(lds0);
        const int tt = 8 * (x >> 1) + (i >> 2), dtile = 4 * (x & 1) + (i & 3);
        const u32 off = w < 2 ? WS_FU + FS * (dtile * 32 + tt) : w == 3 ? WS_FG + FS * (64 * (x >> 1) + 32 * (x & 1) + i) : WS_FW + FS * ((b / 48) * 64 + b % 48);
        if (lane == 0) gst64(p.ws, off, tag);
      }
    }
    if (c == 4) STAMP(3);
  }
  chunk(9, st, 0, T_{});
  STAMP(4);
  const rsrc_t rh = mkrsrc(p.ws + WS_HF, 32 * 48 * 4 * 1024);
  const int m2 = 2 * mt + (w >> 1);
#pragma unroll
  for (int t = 0; t < 2; ++t)
#pragma unroll
    for (int k = 0; k < 3; ++k) {
      const f32x4 a = acc[2 * k][t], c = acc[2 * k + 1][t];
      const u32x4 v = {pk_bf16(fmaxf(a[0], 0.f), fmaxf(a[1], 0.f)), pk_bf16(fmaxf(a[2], 0.f), fmaxf(a[3], 0.f)),
                       pk_bf16(fmaxf(c[0], 0.f), fmaxf(c[1], 0.f)), pk_bf16(fmaxf(c[2], 0.f), fmaxf(c[3], 0.f))};
      bst128<SC1>(rh, v, lane * 16, ((m2 * 48 + 3 * nt + k) * 4 + (w & 1) * 2 + t) * 1024);
    }
}

DEVI void kgen_load(const Params& p, int lane, int d, u32 (&pr)[3][6], u32& ldt, u32& dsk, u64& ep) {
  const u16* src[6] = {p.Are, p.Aim, p.Bre, p.Bim, p.Cre, p.Cim};
#pragma unroll
  for (int s = 0; s < 3; ++s)
#pragma unroll
    for (int a = 0; a < 6; ++a) pr[s][a] = NTP ? __builtin_nontemporal_load(src[a] + d * NM + 64 * s + lane) : src[a][d * NM + 64 * s + lane];
  ldt = p.logdt[d]; dsk = p.Dskip[d]; ep = *(const GAS u64*)(p.ws + WS_EP);
}
DEVI void kgen_wave(const Params& p, u32 lds0, u32 cv, int lane, int d, bool first, const u32 (&pr)[3][6], u32 ldt, u64 ep STAMP_ARG) {
  const u32 g = lane >> 4, c16 = lane & 15;
  const float dt = rbf(__builtin_amdgcn_exp2f(bf2f(ldt) * 1.44269504088896341f));
  if (first) {
    const u64 tag = ep + 1;
    if (lane == 0) { lds_vw32(lds0 + LDS_SYNC + 4 * SY_TAG, (u32)tag); lds_vw32(lds0 + LDS_SYNC + 4 * SY_TAG + 4, (u32)(tag >> 32)); lds_vw32(lds0 + LDS_SYNC + 4 * SY_TAGV, 1); }
  }
  const u32 tw = cv + CV_TAB + 4 * lane;
  const u32 tr = cv + CV_TAB + c16 * 288 + 16 * g;
  f32x4 acc[3] = {f32x4{0, 0, 0, 0}, f32x4{0, 0, 0, 0}, f32x4{0, 0, 0, 0}};
  lds_w32(tw, 0x00003f80u);
  STAMP(1);
  u32 q0[6], q1[6], q2[6];
#pragma unroll
  for (int a = 0; a < 6; ++a) { q0[a] = pr[0][a]; q1[a] = pr[1][a]; q2[a] = pr[2][a]; }
#pragma unroll 1
  for (int s = 0; s < 3; ++s) {
    const u32 a0 = q0[0], a1 = q0[1], a2 = q0[2], a3 = q0[3], a4 = q0[4], a5 = q0[5];
#pragma unroll
    for (int a = 0; a < 6; ++a) { q0[a] = q1[a]; q1[a] = q2[a]; }
    const float xr = bf2f(a0) * dt, xi = bf2f(a1) * dt, br = bf2f(a2) * dt, bi = bf2f(a3) * dt, cr = bf2f(a4), ci = bf2f(a5);
    const float ex = xr * 1.44269504088896341f, rev = xi * 0.15915494309189535f;
    const f32x2 w1 = cexp_pow(ex, rev, 1.f), w8 = cexp_pow(ex, rev, 8.f), w128 = cexp_pow(ex, rev, 128.f);
    const f32x2 gb = {cr * br - ci * bi, -(cr * bi + ci * br)};
    const f32x2 c1 = conj(w1), c128 = conj(w128);
    f32x2 c2, c4, w16, c256, c512, c1024;
    cmul2(c2, c1, c1, c256, c128, c128);
    cmul2(w16, w8, w8, c512, c256, c256);
    cmul2(c4, c2, c2, c1024, c512, c512);
    {
      f32x2 o = w8, e = w16, v = gb, v2;
      lds_w32(tw + 288 * 1, pk2(o));
      lds_w32(tw + 288 * 2, pk2(e));
      lds_w32(tw + 288 * 16, pk2(v));
#pragma unroll
      for (int r = 0; r < 7; ++r) {
        f32x2 on, vn;
        cmul2(on, o, w16, v2, v, c128);
        lds_w32(tw + 288 * (3 + 2 * r), pk2(on));
        lds_w32(tw + 288 * (24 + r), pk2(v2));
        if (r < 6) {
          f32x2 en;
          cmul2(en, e, w16, vn, v, c1);
          lds_w32(tw + 288 * (4 + 2 * r), pk2(en));
          e = en;
        } else {
          f32x2 dummy;
          cmul2(dummy, v, c1, vn, v, c1);
        }
        lds_w32(tw + 288 * (17 + r), pk2(vn));
        o = on; v = vn;
      }
      f32x2 a, bq;
      cmul2(v2, v, c128, a, gb, c256);
      lds_w32(tw + 288 * 31, pk2(v2));
      fence_c();
      u32x4 fb[4];
#pragma unroll
      for (int k = 0; k < 4; ++k) fb[k] = lds_r128(tr + 64 * k);
      u32x4 fa[4];
#pragma unroll
      for (int k = 0; k < 4; ++k) fa[k] = lds_r128(tr + 16 * 288 + 64 * k);
      fence_c();
      cmul2(bq, gb, c512, v, gb, c1024);
      f32x2 t1[16];
      t1[0] = a; t1[8] = bq;
      cmul2(t1[1], a, c2, t1[9], bq, c4);
      cmul2(t1[2], t1[1], c2, t1[4], a, c128);
      cmul2(t1[3], t1[2], c2, t1[5], t1[1], c128);
      cmul2(t1[6], t1[2], c128, t1[7], t1[3], c128);
      cmul2(t1[10], t1[8], c128, t1[11], t1[9], c128);
      cmul2(t1[12], t1[10], c128, t1[13], t1[11], c128);
      cmul2(t1[14], t1[12], c128, t1[15], t1[13], c128);
#pragma unroll
      for (int k = 0; k < 4; ++k) acc[0] = mfma16(fa[k], fb[k], acc[0]);
#pragma unroll
      for (int r = 0; r < 16; ++r) lds_w32(tw + 288 * (16 + r), pk2(t1[r]));
      fence_c();
#pragma unroll
      for (int k = 0; k < 4; ++k) fa[k] = lds_r128(tr + 16 * 288 + 64 * k);
      fence_c();
      f32x2 t2[9];
      t2[0] = v;
      f32x2 dm;
      cmul2(t2[1], v, c128, t2[2], v, c256);
      cmul2(t2[3], t2[1], c256, t2[4], t2[2], c256);
      cmul2(t2[5], t2[3], c256, t2[6], t2[4], c256);
      cmul2(t2[7], t2[5], c256, t2[8], t2[6], c256);
#pragma unroll
      for (int k = 0; k < 4; ++k) acc[1] = mfma16(fa[k], fb[k], acc[1]);
#pragma unroll
      for (int r = 0; r < 9; ++r) lds_w32(tw + 288 * (16 + r), pk2(t2[r]));
      fence_c();
#pragma unroll
      for (int k = 0; k < 4; ++k) fa[k] = lds_r128(tr + 16 * 288 + 64 * k);
      fence_c();
#pragma unroll
      for (int k = 0; k < 4; ++k) acc[2] = mfma16(fa[k], fb[k], acc[2]);
      (void)dm;
    }
  }
  STAMP(4);
  const u32 ke = cv + CV_KE;
  lds_w128(ke + 16 * lane, u32x4{0, 0, 0, 0});
  lds_w128(ke + 1024 + 16 * lane, u32x4{0, 0, 0, 0});
  fence_c();
  const float k512 = rbf(__int_as_float(__builtin_amdgcn_readlane(__float_as_int(acc[1][0]), 32)));
  if (g < 2) {
    const u32 p0 = 256 + 128 * g + 8 * c16;
    float v3 = acc[1][3];
    if (lane == 31) v3 -= k512;
    const u32 a01 = pk_bf16(acc[1][0], acc[1][1]), a23 = pk_bf16(acc[1][2], v3);
    const u32 v0 = a01 & 0xffffu, v1 = a01 >> 16, v2 = a23 & 0xffffu, v3b = a23 >> 16;
    lds_w128(ke + 2 * (544 - p0), u32x4{v3b | (v2 << 16), v2 | (v2 << 16), v1 | (v0 << 16), v0 | (v0 << 16)});
  } else {
#pragma unroll
    for (int r = 0; r < 4; ++r) {
      const u32 pi = 2 * (c16 & 1) + (r & 1), nu = 32 + 16 * (g - 2) + 8 * (r >> 1) + (c16 >> 1);
      lds_w16(ke + 2 * (576 + 64 * pi + 80 - nu), pk_bf16(acc[1][r], 0.f));
    }
  }
  fence_c();
  {
    const u32 lb = 128 * (g >> 1) + 4 * (g & 1) + 8 * c16;
#pragma unroll
    for (int r = 0; r < 4; ++r) lds_w16(ke + 2 * (550 - lb - r), pk_bf16(acc[0][r], 0.f));
  }
  if (g < 2) {
#pragma unroll
    for (int r = 0; r < 4; ++r) {
      const u32 pi = c16 & 1, nu = 64 + 32 * g + 8 * r + (c16 >> 1);
      lds_w16(ke + 2 * (832 + 96 * pi + 144 - nu), pk_bf16(acc[2][r], 0.f));
    }
  } else if (lane == 32) {
    lds_w16(ke + 2 * (832 + 144 - 128), pk_bf16(acc[2][0], 0.f));
  }
  fence_c();
}

DEVI void conv_wave(const Params& p, u32 lds0, u32 cv, int lane, int d, u32 dskip STAMP_ARG) {
  const u32 g = lane >> 4, r16 = lane & 15;
  const u32 ke = cv + CV_KE, ko = cv + CV_KO;
  if (O_CPRIO) __builtin_amdgcn_s_setprio(3);
  const u64 tag0 = get_tag(lds0);
  const u32 fu_off = WS_FU + FS * ((d / 80) * 32 + (lane & 31));
  u64 fu0 = tag0;
  if (O_EARLYPOLL) fu0 = gld64(p.ws, fu_off);
  if (O_CPRIO) __builtin_amdgcn_s_setprio(PRIO_K);
  {
    u32 a[8], b[8];
#pragma unroll
    for (int j = 0; j < 8; ++j) { a[j] = lds_r32(ke + 4 * (64 * j + lane)); b[j] = lds_r32(ke + 4 * (64 * j + lane) + 4); }
#pragma unroll
    for (int j = 0; j < 8; ++j) lds_w32(ko + 4 * (64 * j + lane), __builtin_amdgcn_alignbit(b[j], a[j], 16));
  }
  sy_wait_ge(lds0 + LDS_SYNC + 4 * SY_SCR, 4);
  const u32 ub = cv + CV_U + 512;
  if (lane < 32) lds_w128(cv + CV_U + 16 * lane, u32x4{0, 0, 0, 0});
  else if (lane < 48) {
    const u32 arr = (lane >> 2) & 3;
    const u32 base = arr == 0 ? CV_W5 : arr == 1 ? CV_W3 : arr == 2 ? CV_W9 : CV_W7;
    lds_w128(cv + base + 16 * (lane & 3), u32x4{0, 0, 0, 0});
  }
  if (O_CPRIO) __builtin_amdgcn_s_setprio(3);
  if (!O_EARLYPOLL || !__all(lane >= 32 || fu0 == tag0)) poll_flags<FU_SLEEP>(p.ws, fu_off, lane < 32, tag0);
  STAMP(7);
  const rsrc_t rut = mkrsrc(p.ws + WS_UT, D * L * 2);
  u32x4 uv[4];
#pragma unroll
  for (int j = 0; j < 4; ++j) uv[j] = bld128<SC1>(rut, d * 4096 + 1024 * j + 16 * lane, 0);
  SB();
  if (O_CPRIO) __builtin_amdgcn_s_setprio(PRIO_K);
  const u32 kpar = (r16 & 1) ? ko - 2 : ke;
  u32x4 A[17];
  {
    const u32 ab = kpar + 2 * (550 - 32 * 16 - 16 * (g >> 1) + 8 * (g & 1) - r16);
#pragma unroll
    for (int k = 0; k < 17; ++k) {
      const u32 a = ab + 64 * (16 - k);
      A[k] = u32x4{lds_r32(a), lds_r32(a + 4), lds_r32(a + 8), lds_r32(a + 12)};
    }
  }
  SB();
#pragma unroll
  for (int j = 0; j < 4; ++j) lds_w128(ub + 1024 * j + 16 * lane, uv[j]);
  fence_c();
  STAMP(8);
  {
    u32x2 pv[3], nx[3]; u32x4 cu[3];
#pragma unroll
    for (int it = 0; it < 3; ++it) {
      const u32 a = ub + 1024 * it + 16 * lane;
      pv[it] = lds_r64(a - 8); cu[it] = lds_r128(a); nx[it] = lds_r64(a + 16);
    }
    const u32x2 u0 = lds_r64(ub);
#pragma unroll
    for (int it = 0; it < 3; ++it) {
      float v[16];
      v[0] = bflo(pv[it].x); v[1] = bfhi(pv[it].x); v[2] = bflo(pv[it].y); v[3] = bfhi(pv[it].y);
#pragma unroll
      for (int e = 0; e < 4; ++e) { v[4 + 2 * e] = bflo(cu[it][e]); v[5 + 2 * e] = bfhi(cu[it][e]); }
      v[12] = bflo(nx[it].x); v[13] = bfhi(nx[it].x); v[14] = bflo(nx[it].y); v[15] = bfhi(nx[it].y);
      float w3[8], w5[8], w7[8], w9[8];
#pragma unroll
      for (int e = 0; e < 8; ++e) {
        w3[e] = v[4 + e] + (v[3 + e] + v[5 + e]);
        w5[e] = w3[e] + (v[2 + e] + v[6 + e]);
        if (it < 2) { w7[e] = w5[e] + (v[1 + e] + v[7 + e]); w9[e] = w7[e] + (v[e] + v[8 + e]); }
      }
      const u32 o = 64 + 1024 * it + 16 * lane;
      lds_w128(cv + CV_W5 + o, u32x4{pk_bf16(w5[0], w5[1]), pk_bf16(w5[2], w5[3]), pk_bf16(w5[4], w5[5]), pk_bf16(w5[6], w5[7])});
      lds_w128(cv + CV_W3 + o, u32x4{pk_bf16(w3[0], w3[1]), pk_bf16(w3[2], w3[3]), pk_bf16(w3[4], w3[5]), pk_bf16(w3[6], w3[7])});
      if (it < 2) {
        lds_w128(cv + CV_W9 + o, u32x4{pk_bf16(w9[0], w9[1]), pk_bf16(w9[2], w9[3]), pk_bf16(w9[4], w9[5]), pk_bf16(w9[6], w9[7])});
        lds_w128(cv + CV_W7 + o, u32x4{pk_bf16(w7[0], w7[1]), pk_bf16(w7[2], w7[3]), pk_bf16(w7[4], w7[5]), pk_bf16(w7[6], w7[7])});
      }
    }
    fence_c();
    const float a0 = bflo(u0.x), a1 = a0 + bfhi(u0.x), a2 = a1 + bflo(u0.y), a3 = a2 + bfhi(u0.y);
    if (lane == 0) {
      lds_w64(cv + CV_W9 + 64 - 8, u32x2{pk_bf16(a0, a1), pk_bf16(a2, a3)});
      lds_w64(cv + CV_W7 + 64 - 8, u32x2{pk_bf16(0.f, a0), pk_bf16(a1, a2)});
      lds_w64(cv + CV_W5 + 64 - 8, u32x2{0u, pk_bf16(a0, a1)});
      lds_w64(cv + CV_W3 + 64 - 8, u32x2{0u, pk_bf16(0.f, a0)});
    }
  }
  fence_c();
  if (PRIO_NEAR) __builtin_amdgcn_s_setprio(PRIO_NEAR);
  STAMP(9);
  f32x4 acc[8];
#pragma unroll
  for (int t = 0; t < 8; ++t) acc[t] = f32x4{0, 0, 0, 0};
  {
    const u32 bb = ub + 2 * (16 * r16 - 16 * (g >> 1) + 8 * (g & 1));
    const u32 kc = (*(LAS u16*)(uintptr_t)(ke + 2 * (832 + 144 - 64))) ^ 0x8000u;
    const int idx0 = 1008 + 16 * (g >> 1) - 8 * (g & 1) + r16;
    u32x4 C;
#pragma unroll
    for (int e = 0; e < 4; ++e) {
      const int i0 = idx0 - 2 * e, i1 = i0 - 1;
      const u32 lo = (i0 == 1020 || i0 == 1021) ? kc : 0u, hi = (i1 == 1020 || i1 == 1021) ? kc : 0u;
      C[e] = lo | (hi << 16);
    }
    constexpr int PF = 6;
    u32x4 B[PF];
#pragma unroll
    for (int j = 0; j < PF; ++j) B[j] = lds_r128(bb + 64 * (j - 7));
    SB();
#pragma unroll
    for (int z = -7; z <= 56; ++z) {
      const int sl = (z + 7) % PF;
#pragma unroll
      for (int t = 0; t < 8; ++t) {
        const int k = 8 * t - z;
        const int kmax = t == 0 ? 7 : (t == 1 ? 15 : 16);
        if (k >= 0 && k <= kmax) acc[t] = mfma16(A[k], B[sl], acc[t]);
      }
      if (z + PF <= 56) B[sl] = lds_r128(bb + 64 * (z + PF));
      else if (z + PF - 57 < 5) B[sl] = lds_r128(bb + 2 * (256 * (z + PF - 57 + 3) - 16 * 63));
      SB();
    }
#pragma unroll
    for (int t = 3; t < 8; ++t) acc[t] = mfma16(C, B[(57 + t - 3 + 7) % PF], acc[t]);
  }
  STAMP(10);
  const u32 zero_a = cv + CV_U + 8 * (lane & 3);
  const u32 q = r16 >> 2, pp = lane & 3;
  {
    u32x4 T4[2][3], T8[5];
#pragma unroll
    for (int pr = 0; pr < 2; ++pr)
#pragma unroll
      for (int dl = 0; dl < 3; ++dl) {
        const u32 a = kpar + 2 * (576 + 64 * (2 * pr + (g >> 1)) + 80 - 16 * (dl + 2) - r16 + 4 * (g & 1));
        T4[pr][dl] = u32x4{lds_r32(a), lds_r32(a + 4), lds_r32(a + 16), lds_r32(a + 20)};
      }
#pragma unroll
    for (int dl = 0; dl < 5; ++dl) {
      const u32 a = kpar + 2 * (832 + 96 * (g >> 1) + 144 - 16 * (dl + 4) - r16 + 4 * (g & 1));
      T8[dl] = u32x4{lds_r32(a), lds_r32(a + 4), lds_r32(a + 16), lds_r32(a + 20)};
    }
    const u32 wb4 = cv + ((g >> 1) ? CV_W3 - 8 : CV_W5) + 64 + 2 * (16 * (4 * (g & 1) + q) + 4 * pp);
    const u32 wb8 = cv + ((g >> 1) ? CV_W7 - 16 : CV_W9) + 64 + 2 * (16 * (4 * (g & 1) + q) + 4 * pp);
    const bool edge = (g & 1) && q == 3;
    u32x4 A4[2][7], A8[5];
#pragma unroll
    for (int pr = 0; pr < 2; ++pr)
#pragma unroll
      for (int be = -1; be <= 5; ++be) {
        if (be == -1) {
          if (pr == 0) { const u32x2 hi = lds_tr(edge ? wb4 - 512 + 256 : zero_a); A4[0][0] = u32x4{0u, 0u, hi.x, hi.y}; }
        } else {
          const u32 a = wb4 + 512 * be - 16 * pr;
          A4[pr][be + 1] = cat(lds_tr(a), lds_tr(a + 256));
        }
      }
#pragma unroll
    for (int be = -1; be <= 3; ++be) {
      if (be == -1) { const u32x2 hi = lds_tr(edge ? wb8 - 512 + 256 : zero_a); A8[0] = u32x4{0u, 0u, hi.x, hi.y}; }
      else { const u32 a = wb8 + 512 * be; A8[be + 1] = cat(lds_tr(a), lds_tr(a + 256)); }
    }
#pragma unroll
    for (int be = -1; be <= 5; ++be)
#pragma unroll
      for (int pr = 0; pr < 2; ++pr) {
        if (be == -1 && pr == 1) continue;
#pragma unroll
        for (int dl = 0; dl < 3; ++dl) {
          const int t = be + dl + 2;
          if (t >= 1 && t <= 7) acc[t] = mfma16(A4[pr][be + 1], T4[pr][dl], acc[t]);
        }
      }
#pragma unroll
    for (int be = -1; be <= 3; ++be)
#pragma unroll
      for (int dl = 0; dl < 5; ++dl) {
        const int t = be + dl + 4;
        if (t >= 3 && t <= 7) acc[t] = mfma16(A8[be + 1], T8[dl], acc[t]);
      }
  }
  STAMP(11);
  fence_c();
  const float ds = bf2f(dskip);
  u32x2 uu[8];
#pragma unroll
  for (int t = 0; t < 8; ++t) uu[t] = lds_r64(ub + 2 * (256 * t + 16 * r16 + 4 * g));
  const rsrc_t rat = mkrsrc(p.ws + WS_AT, D * L * 2);
  if (O_CPRIO) __builtin_amdgcn_s_setprio(3);
#pragma unroll
  for (int t = 0; t < 8; t += 2) {
    u32x2 av[2];
#pragma unroll
    for (int h = 0; h < 2; ++h) {
      const f32x4 y = acc[t + h];
      const u32x2 u2 = uu[t + h];
      const u32 y01 = pk_bf16(y[0], y[1]), y23 = pk_bf16(y[2], y[3]);
      const u32 s01 = pk_bf16(bflo(u2.x) * ds, bfhi(u2.x) * ds), s23 = pk_bf16(bflo(u2.y) * ds, bfhi(u2.y) * ds);
      av[h] = u32x2{pk_bf16(bflo(y01) + bflo(s01), bfhi(y01) + bfhi(s01)), pk_bf16(bflo(y23) + bflo(s23), bfhi(y23) + bfhi(s23))};
    }
    const auto s0 = __builtin_amdgcn_permlane16_swap(av[0].x, av[1].x, false, false);
    const auto s1 = __builtin_amdgcn_permlane16_swap(av[0].y, av[1].y, false, false);
    bst128<SC1>(rat, u32x4{s0[0], s1[0], s0[1], s1[1]}, d * 4096 + 2 * (256 * (t + (g & 1)) + 16 * r16 + 8 * (g >> 1)), 0);
  }
  const u64 tag = get_tag(lds0);
  wait_vm0();
  if (lane == 0) gst64(p.ws, WS_FA + FS * d, tag);
}

#define WPF 6
constexpr int WP = WPF;
template <int S0, int S1>
DEVI void gemm2_prefetch(const Params& p, int kw, int lane, int j, u32x4 (&wf)[WP][5]) {
  const rsrc_t rw = mkrsrc(p.ws + WS_W2F, 8 * 48 * 5 * 1024);
  const u32 vo = lane * 16, wb = (j * 48 + 12 * kw) * 5120;
#pragma unroll
  for (int s = S0; s < S1; ++s)
#pragma unroll
    for (int n = 0; n < 5; ++n) wf[s][n] = bld128<SC1>(rw, vo + 1024 * (n & 3), wb + s * 5120 + (n >> 2) * 4096);
}
DEVI void gemm2_main(const Params& p, u32 lds0, int kw, int lane, int m2, int j, u32x4 (&wf)[WP][5], f32x4 (&acc)[4][5]) {
  const rsrc_t rh = mkrsrc(p.ws + WS_HF, 32 * 48 * 4 * 1024), rw = mkrsrc(p.ws + WS_W2F, 8 * 48 * 5 * 1024);
  const u32 vo = lane * 16;
  const u32 hb = (m2 * 48 + 12 * kw) * 4096, wb = (j * 48 + 12 * kw) * 5120;
  constexpr int DP = 2;
  u32x4 hf[DP][4];
#pragma unroll
  for (int s = 0; s < DP; ++s)
#pragma unroll
    for (int m = 0; m < 4; ++m) hf[s][m] = bld128<SC1>(rh, vo + 1024 * m, hb + s * 4096);
#pragma unroll
  for (int s = 0; s < 12; ++s) {
    const int sl = s % DP, sw = s % WP;
    if (s == G2SIG && kw == 0) sy_set(lds0 + LDS_SYNC + 4 * SY_G2, 1, lane);
#pragma unroll
    for (int n = 0; n < 5; ++n) {
#pragma unroll
      for (int m = 0; m < 4; ++m) {
        if (s == 0) mfma16z(acc[m][n], wf[sw][n], hf[sl][m]); else mfma16t(acc[m][n], wf[sw][n], hf[sl][m]);
        if (n == 4 && s + DP < 12) { SB(); hf[sl][m] = bld128<SC1>(rh, vo + 1024 * m, hb + (s + DP) * 4096); SB(); }
      }
      if (s + WP < 12) { SB(); wf[sw][n] = bld128<SC1>(rw, vo + 1024 * (n & 3), wb + (s + WP) * 5120 + (n >> 2) * 4096); }
      SB();
    }
  }
  asm volatile("s_nop 7\n\ts_nop 3" : "+v"(acc[0][4]), "+v"(acc[1][4]), "+v"(acc[2][4]), "+v"(acc[3][4]), "+v"(acc[0][3]), "+v"(acc[1][3]), "+v"(acc[2][3]), "+v"(acc[3][3]) :: "memory");
  asm volatile("" : "+v"(acc[0][0]), "+v"(acc[1][0]), "+v"(acc[2][0]), "+v"(acc[3][0]), "+v"(acc[0][1]), "+v"(acc[1][1]), "+v"(acc[2][1]), "+v"(acc[3][1]), "+v"(acc[0][2]), "+v"(acc[1][2]), "+v"(acc[2][2]), "+v"(acc[3][2]) :: "memory");
}

__global__ __launch_bounds__(512, 2) void fused_kernel(Params p) {
  extern __shared__ __attribute__((aligned(16))) char smem[];
  const u32 lds0 = (u32)(uintptr_t)(LAS char*)smem;
  const int b = blockIdx.x, lane = threadIdx.x & 63, w = rfl(threadIdx.x >> 6);
  const int x = b & 7, i = b >> 3;
  const u32 sy = lds0 + LDS_SYNC;
  const int mt = 4 * (x >> 1) + (i >> 3), nt = 8 * (x & 1) + (i & 7);
  const int m2 = 4 * x + (i >> 3), j = i & 7;
  STAMP(0);
  if (O_CODEPF && w == 0) {
    u64 pc; u32 sz;
    asm volatile("s_getpc_b64 %0\n.Lfused_code_begin:" : "=s"(pc));
    asm volatile("s_mov_b32 %0, .Lfused_code_end-.Lfused_code_begin" : "=s"(sz));
    const u32 off = (u32)(12 * i + lane) * 128;
    if (lane < 12 && off < sz) {
      const char* src = (const char*)(pc & ~127ull) + off;
      asm volatile("s_mov_b32 m0, %0\n\ts_nop 0\n\tglobal_load_lds_dword %1, off" :: "s"(lds0 + LDS_JUNK), "v"(src) : "memory");
    }
  }
  if (O_TLB && w == 1) {
    const rsrc_t rws = mkrsrc(p.ws, WS_END), ro = mkrsrc(p.out, L * D * 2);
    constexpr u32 NW = O_TLB ? (WS_END + O_TLB - 1) / (O_TLB ? O_TLB : 1) : 0, NO = O_TLB ? (L * D * 2 + O_TLB - 1) / (O_TLB ? O_TLB : 1) + 1 : 0;
#pragma unroll 1
    for (u32 q = lane; q < NW + NO + 63 - (NW + NO + 63) % 64; q += 64) {
      const bool isw = q < NW;
      const u32 off = isw ? q * O_TLB : min((q - NW) * O_TLB, (u32)(L * D * 2 - 4));
      if (isw) asm volatile("s_mov_b32 m0, %0\n\ts_nop 0\n\tbuffer_load_dword %1, %2, 0 offen lds" :: "s"(lds0 + LDS_JUNK), "v"(off), "s"(rws) : "memory");
      else if (q < NW + NO) asm volatile("s_mov_b32 m0, %0\n\ts_nop 0\n\tbuffer_load_dword %1, %2, 0 offen lds" :: "s"(lds0 + LDS_JUNK), "v"(off), "s"(ro) : "memory");
    }
  }
  if (threadIdx.x < 16) lds_w32(sy + 4 * threadIdx.x, 0);
  const int xw = w - 4, nch = b < 128 ? 3 : 2, d0 = b < 128 ? 3 * b : 384 + 2 * (b - 128);
  u32 kpr[3][6], kldt = 0, kdsk = 0; u64 kep = 0;
#pragma unroll
  for (int s = 0; s < 3; ++s)
#pragma unroll
    for (int a = 0; a < 6; ++a) kpr[s][a] = 0;
  if (O_KPRE && w >= 4 && w < 7 && xw < nch) kgen_load(p, lane, d0 + xw, kpr, kldt, kdsk, kep);
  if (!(PREBAR && w == 7)) {
    wait_lgkm0();
    __builtin_amdgcn_s_barrier();
    fence_c();
  }
  f32x4 acc[4][5];
  if (w < 4) {
    __builtin_amdgcn_s_setprio(PRIO_G);
    gemm1_wave(p, lds0, w, lane, mt, nt, b STAMP_PASS);
    STAMP(5);
    u32x4 g2[WP][5];
    const u64 tag = get_tag(lds0);
    sy_wait_ge(sy + 4 * SY_AUX, 1);
    gemm2_prefetch<0, 1>(p, w, lane, j, g2);
    SB();
    asm volatile("s_waitcnt vmcnt(5)" ::: "memory");
    if (lane == 0) gst64(p.ws, WS_FH + FS * (((2 * mt + (w >> 1)) * 16 + nt) * 2 + (w & 1)), tag);
    STAMP(7);
    SB();
    const u32 fho = WS_FH + FS * (m2 * 32 + 8 * w + (lane & 7));
    gemm2_prefetch<1, HPOLL>(p, w, lane, j, g2);
    SB();
    const u64 fh0 = gld64(p.ws, fho);
    SB();
    gemm2_prefetch<HPOLL, WP>(p, w, lane, j, g2);
    SB();
    if (!__all(fh0 == tag)) poll_flags(p.ws, fho, lane < 8, tag);
    fence_c();
    STAMP(6);
    gemm2_main(p, lds0, w, lane, m2, j, g2, acc);
    STAMP(13);
#pragma unroll
    for (int m = 0; m < 4; ++m) {
      if (m == w) continue;
      const u32 slot = (w - m - 1) & 3;
#pragma unroll
      for (int n = 0; n < 5; ++n) lds_w128(lds0 + LDS_RED + ((m * 3 + slot) * 5 + n) * 1024 + 16 * lane, __builtin_bit_cast(u32x4, acc[m][n]));
    }
  } else if (w == 7) {
    __builtin_amdgcn_s_setprio(PRIO_L);
    gemm1_loader(p, lds0, lane, mt, nt, ROT1 == 1 ? ((i & 7) + 3 * (i >> 3)) % 10 : ROT1 == 2 ? ((i & 7) + (i >> 3)) & 3 : ROT1 == 3 ? ((i & 7) + (i >> 3)) % 5 : ROT1 == 4 ? ((i & 7) + (i >> 3)) & 1 : 0 STAMP_PASS);
    STAMP(5);
    const u64 tag = get_tag(lds0);
    {
      const int ca = (80 * j) / 128, cb = (80 * j + 79) / 128;
      poll_flags(p.ws, WS_FW + FS * (ca * 64 + lane), lane < 48, tag);
      if (cb != ca) poll_flags(p.ws, WS_FW + FS * (cb * 64 + lane), lane < 48, tag);
    }
    sy_set(sy + 4 * SY_AUX, 1, lane);
    STAMP(6);
    sy_wait_ge(sy + 4 * SY_G2, 1);
    STAMP(7);
    {
      const u32 o0 = WS_FA + FS * (80 * j + lane);
      const u32 o1 = lane < 16 ? WS_FA + FS * (80 * j + 64 + lane) : WS_FG + FS * (m2 * 8 + (lane & 7));
      for (u32 n = 0; n < POLL_CAP; ++n) {
        const u64 v0 = gld64(p.ws, o0), v1 = lane < 24 ? gld64(p.ws, o1) : tag;
        if (__all(v0 == tag && v1 == tag)) break;
        __builtin_amdgcn_s_sleep(FA_SLEEP);
      }
      fence_c();
    }
    STAMP(8);
    const rsrc_t rat = mkrsrc(p.ws + WS_AT, D * L * 2);
    u32x4 av[10];
#pragma unroll
    for (int jj = 0; jj < 10; ++jj) {
      const u32 id = 64 * jj + lane, row = id >> 3, c = id & 7;
      av[jj] = bld128<SC1>(rat, (80 * j + row) * 4096 + 128 * m2 + 16 * c, 0);
    }
    const float gv = __hip_atomic_load((GAS float*)(p.ws + WS_G) + 64 * m2 + lane, RLX_AGENT);
#pragma unroll
    for (int jj = 0; jj < 10; ++jj) {
      const u32 id = 64 * jj + lane, row = id >> 3, c = id & 7;
      lds_w128(lds0 + LDS_ATILE + row * 160 + 16 * c, av[jj]);
    }
    lds_w32(lds0 + LDS_GT + 4 * lane, __float_as_uint(gv));
    STAMP(13);
  } else {
    __builtin_amdgcn_s_setprio(PRIO_K);
    if (xw < nch) {
      const u32 cv = lds0 + LDS_CONV + xw * CV_SZ;
      if (!O_KPRE) kgen_load(p, lane, d0 + xw, kpr, kldt, kdsk, kep);
      kgen_wave(p, lds0, cv, lane, d0 + xw, xw == 0, kpr, kldt, kep STAMP_PASS);
      STAMP(5);
      conv_wave(p, lds0, cv, lane, d0 + xw, kdsk STAMP_PASS);
      STAMP(13);
    }
  }
  wait_lgkm0();
  __builtin_amdgcn_s_barrier();
  fence_c();
  STAMP(14);
  if (w < 4) {
    const u32 g = lane >> 4, r16 = lane & 15;
    f32x4 own[5];
#pragma unroll
    for (int m = 0; m < 4; ++m)
      if (m == w) {
#pragma unroll
        for (int n = 0; n < 5; ++n) own[n] = acc[m][n];
      }
    const int t = 64 * m2 + 16 * w + r16;
    const float gv = __uint_as_float(lds_r32(lds0 + LDS_GT + 4 * (16 * w + r16)));
    const float og = rbf(1.f - gv);
    const rsrc_t ro = mkrsrc(p.out, L * D * 2);
    u32x2 ov[5];
#pragma unroll
    for (int n = 0; n < 5; ++n) {
#pragma unroll
      for (int s = 0; s < 3; ++s) own[n] += __builtin_bit_cast(f32x4, lds_r128(lds0 + LDS_RED + ((w * 3 + s) * 5 + n) * 1024 + 16 * lane));
      const u32x2 av = lds_tr(lds0 + LDS_ATILE + (16 * n + 4 * g + (r16 >> 2)) * 160 + 32 * w + 8 * (lane & 3));
      const u32 b01 = pk_bf16(own[n][0], own[n][1]), b23 = pk_bf16(own[n][2], own[n][3]);
      const u32 ga01 = pk_bf16(gv * bflo(av.x), gv * bfhi(av.x)), ga23 = pk_bf16(gv * bflo(av.y), gv * bfhi(av.y));
      const u32 gb01 = pk_bf16(og * bflo(b01), og * bfhi(b01)), gb23 = pk_bf16(og * bflo(b23), og * bfhi(b23));
      ov[n] = u32x2{pk_bf16(bflo(ga01) + bflo(gb01), bfhi(ga01) + bfhi(gb01)), pk_bf16(bflo(ga23) + bflo(gb23), bfhi(ga23) + bfhi(gb23))};
      if (!O_WIDEOUT || n == 4) {
        if (n == 0) STAMP(12);
        bst64<OUTPOL>(ro, ov[n], t * 1280 + (80 * j + 16 * n + 4 * g) * 2, 0);
      } else if (n & 1) {
        const auto s0 = __builtin_amdgcn_permlane16_swap(ov[n - 1].x, ov[n].x, false, false);
        const auto s1 = __builtin_amdgcn_permlane16_swap(ov[n - 1].y, ov[n].y, false, false);
        if (n == 1) STAMP(12);
        bst128<OUTPOL>(ro, u32x4{s0[0], s1[0], s0[1], s1[1]}, t * 1280 + (80 * j + 16 * (n - 1 + (g & 1)) + 8 * (g >> 1)) * 2, 0);
      }
    }
    if (b == 0 && w == 0) { const u64 tag = get_tag(lds0); if (lane == 0) gst64(p.ws, WS_EP, tag); }
  }
  asm volatile(".Lfused_code_end:");
}

extern "C" void kernel_launch(void* const* d_in, const int* in_sizes, int n_in, void* d_out, int out_size, void* d_ws, size_t ws_size,
                              hipStream_t stream) {
  Params p;
  p.u = (const u16*)d_in[0]; p.Are = (const u16*)d_in[1]; p.Aim = (const u16*)d_in[2]; p.Bre = (const u16*)d_in[3];
  p.Bim = (const u16*)d_in[4]; p.Cre = (const u16*)d_in[5]; p.Cim = (const u16*)d_in[6]; p.logdt = (const u16*)d_in[7];
  p.Dskip = (const u16*)d_in[8]; p.w1 = (const u16*)d_in[10]; p.w2 = (const u16*)d_in[11]; p.gw = (const u16*)d_in[12];
  p.out = (u16*)d_out; p.ws = (char*)d_ws;
  (void)hipFuncSetAttribute((const void*)fused_kernel, hipFuncAttributeMaxDynamicSharedMemorySize, LDS_TOTAL);
  fused_kernel<<<256, 512, LDS_TOTAL, stream>>>(p);
}
```
